```python
import torch

def _softmax(x, axis=-1):
    return torch.softmax(x, dim=axis)

def _gelu(x):
    return torch.nn.functional.gelu(x, approximate='tanh')

def _silu(x):
    return torch.nn.functional.silu(x)

def _sigmoid(x):
    return torch.sigmoid(x)

def _layernorm(x, axis=-1, eps=1e-05):
    m = x.mean(dim=axis, keepdim=True)
    v = x.var(dim=axis, keepdim=True, unbiased=False)
    return (x - m) / torch.sqrt(v + eps)

def _scatter_add(tgt, idx, src, axis=0):
    return tgt.clone().index_add_(axis, idx, src)

def _segment_sum(src, seg, n):
    out = torch.zeros((n,) + src.shape[1:], dtype=src.dtype)
    return out.index_add_(0, seg, src)

def _pad(x, pw):
    flat = tuple((v for pair in reversed(pw) for v in pair))
    return torch.nn.functional.pad(x, flat)
kernel_name = 'gated_mix__sliding_block__memory_aug_recurrent_mem_tr__prompt'

def setup_inputs():

    _w = lambda a: (lambda t: t.to(torch.bfloat16) if t.is_floating_point() else t)(torch.as_tensor(a))

    def _A_setup():
        B, S, D = (1, 2048, 448)
        return {'x': _w((lambda *s: torch.randn(*s, dtype=torch.bfloat16) * 0.05)(B, S, D)), 'wqkv': _w((lambda *s: torch.randn(*s, dtype=torch.bfloat16) * 1.6)(D, 3 * D)), 'wo': _w((lambda *s: torch.randn(*s, dtype=torch.bfloat16) * 0.05)(D, D))}
    _A_in = _A_setup()

    def _B_setup():
        B, S, Dm = (1, 2048, 448)
        H, Dh = (16, 256)
        M, seg = (8, 32)
        seg = min(seg, S)
        S = S // seg * seg
        return {'x': _w((lambda *s: torch.randn(*s, dtype=torch.bfloat16) * 0.05)(B, S, Dm)), 'mem0': _w((lambda *s: torch.randn(*s, dtype=torch.bfloat16) * 1.6)(B, M, Dm)), 'Wq': _w((lambda *s: torch.randn(*s, dtype=torch.bfloat16) * 0.8)(Dm, H, Dh) * Dm ** (-0.5)), 'Wk': _w((lambda *s: torch.randn(*s, dtype=torch.bfloat16) * 0.8)(Dm, H, Dh) * Dm ** (-0.5)), 'Wv': _w((lambda *s: torch.randn(*s, dtype=torch.bfloat16) * 1.6)(Dm, H, Dh) * Dm ** (-0.5)), 'Wo': _w((lambda *s: torch.randn(*s, dtype=torch.bfloat16) * 1.6)(H, Dh, Dm) * (H * Dh) ** (-0.5)), 'seg_len': seg}
    _B_in = _B_setup()
    return {'A_x': _A_in['x'], 'A_wqkv': _A_in['wqkv'], 'A_wo': _A_in['wo'], 'B_x': _B_in['x'], 'B_mem0': _B_in['mem0'], 'B_Wq': _B_in['Wq'], 'B_Wk': _B_in['Wk'], 'B_Wv': _B_in['Wv'], 'B_Wo': _B_in['Wo'], 'B_seg_len': _B_in['seg_len'], 'gate_w': _w((lambda *s: torch.randn(*s, dtype=torch.bfloat16) * 0.05)(448) * 16.0)}

def reference(A_x, A_wqkv, A_wo, B_x, B_mem0, B_Wq, B_Wk, B_Wv, B_Wo, B_seg_len, gate_w):

    def _A_ref(x, wqkv, wo):
        B, S, D = x.shape
        H = 16
        Dh = D // H
        W = 256
        qkv = (lambda x, *d: x.permute(*d))((x @ wqkv).reshape(B, S, 3, H, Dh), 2, 0, 3, 1, 4)
        q, k, v = (qkv[0], qkv[1], qkv[2])
        scores = torch.einsum('bhqd,bhkd->bhqk', q, k) * Dh ** (-0.5)
        idx = torch.arange(S)
        mask = (idx[:, None] - idx[None, :] >= 0) & (idx[:, None] - idx[None, :] < W)
        scores = torch.where(mask[None, None], scores, -1000000000.0)
        p = _softmax(scores, axis=-1)
        o = torch.einsum('bhqk,bhkd->bhqd', p, v)
        return (lambda x, *d: x.permute(*d))(o, 0, 2, 1, 3).reshape(B, S, D) @ wo

    def _B_ref(x, mem0, Wq, Wk, Wv, Wo, seg_len):
        B, S, Dm = x.shape
        H, Dh = (Wq.shape[1], Wq.shape[2])
        M = mem0.shape[1]
        scale = Dh ** (-0.5)
        n_seg = S // seg_len
        L = M + seg_len
        idx = torch.arange(L)
        mask = (idx[:, None] < M) | (idx[:, None] >= idx[None, :])
        mem = mem0
        outs = []
        for s in range(n_seg):
            xs = x[:, s * seg_len:(s + 1) * seg_len, :].clone()
            h = torch.concatenate([mem, xs], axis=1)
            q = torch.einsum('btd,dhe->bhte', h, Wq)
            k = torch.einsum('btd,dhe->bhte', h, Wk)
            v = torch.einsum('btd,dhe->bhte', h, Wv)
            scores = torch.einsum('bhqe,bhke->bhqk', q, k) * scale
            scores = torch.where(mask[None, None], scores, -1000000000.0)
            p = _softmax(scores, axis=-1)
            o = torch.einsum('bhqk,bhke->bhqe', p, v)
            o = torch.einsum('bhte,hed->btd', o, Wo)
            mem = o[:, :M, :].clone()
            outs.append(o[:, M:, :])
        return torch.concatenate(outs, axis=1)
    x = A_x
    g = _sigmoid((x * gate_w).sum(-1, keepdims=True))
    a = _A_ref(x, A_wqkv, A_wo)
    b = _B_ref(x, B_mem0, B_Wq, B_Wk, B_Wv, B_Wo, B_seg_len)
    return g * a + (1 - g) * b
```

```cpp
#include <hip/hip_runtime.h>
#include <hip/hip_bf16.h>
#include <stdint.h>

typedef __bf16 bf8 __attribute__((ext_vector_type(8)));
typedef __bf16 bf2 __attribute__((ext_vector_type(2)));
typedef float f4 __attribute__((ext_vector_type(4)));
typedef unsigned u4 __attribute__((ext_vector_type(4)));
typedef unsigned u2 __attribute__((ext_vector_type(2)));
typedef short s4 __attribute__((ext_vector_type(4)));
#define LAS __attribute__((address_space(3)))
#define SENT 0xFFFFFFFFu

constexpr int NSEG = 64;
constexpr int SPIN_MAX = 1 << 14;
#define PROBE_AT 380ull
#define PROBE_REP 2
constexpr unsigned OFF_HOP = 0;
constexpr unsigned HOP_BYTES = 13u * 524288u;
constexpr unsigned OFF_CTRL = OFF_HOP + HOP_BYTES;
constexpr unsigned CTRL_BYTES = 65536;
constexpr unsigned OFF_MEMC = OFF_CTRL + CTRL_BYTES;
constexpr unsigned MEMC_BYTES = 64u * 7168u;
constexpr unsigned INIT_BYTES = HOP_BYTES + CTRL_BYTES + MEMC_BYTES;
constexpr unsigned W_BYTES = 16u * 28u * 7u * 2048u;
constexpr unsigned OFF_AW = INIT_BYTES;
constexpr unsigned OFF_BW = OFF_AW + W_BYTES;
constexpr unsigned OFF_XK = OFF_BW + W_BYTES;
constexpr unsigned OFF_ZX = OFF_XK + 64u * 32768u;
constexpr unsigned OFF_WF = OFF_ZX + 64u * 28u * 16384u;
constexpr unsigned OFF_WO = OFF_WF + 96u * 14u * 1024u;
constexpr unsigned OFF_GT = OFF_WO + 28u * 16u * 1024u;
constexpr unsigned OFF_QF = OFF_GT + 8192u;
constexpr unsigned OFF_KF = OFF_QF + 128u * 16u * 1024u;
constexpr unsigned OFF_VF = OFF_KF + 128u * 16u * 1024u;
constexpr unsigned OFF_AF = OFF_VF + 128u * 16u * 1024u;
constexpr unsigned OFF_PX = OFF_AF + 64u * 28u * 2048u;
constexpr unsigned OFF_PM = OFF_PX + 64u * 32768u;
constexpr unsigned OFF_EP = OFF_PM + 64u * 8192u;
constexpr unsigned OFF_DBG = OFF_EP + 4096u;
constexpr unsigned CT_PRB = OFF_CTRL + 36864;
constexpr unsigned CT_INIT = OFF_CTRL + 512;
constexpr unsigned CT_ROT = OFF_CTRL + 768;
constexpr unsigned CT_AB = OFF_CTRL + 20480;
constexpr unsigned CT_ZF = OFF_CTRL + 1024;
constexpr unsigned CT_XF = OFF_CTRL + 8192;
constexpr unsigned CT_QF = OFF_CTRL + 12288;
constexpr unsigned CT_AF = OFF_CTRL + 16384;
constexpr unsigned CT_WF = OFF_CTRL + 12544;

struct Params {
    const unsigned short* x; const unsigned short* wqkv; const unsigned short* wo; const unsigned short* mem0;
    const unsigned short* Wq; const unsigned short* Wk; const unsigned short* Wv; const unsigned short* Wo;
    const unsigned short* gate; unsigned short* out; unsigned char* ws;
};

__device__ __forceinline__ unsigned pk2(float a, float b) { bf2 v; v[0] = (__bf16)a; v[1] = (__bf16)b; return __builtin_bit_cast(unsigned, v); }
__device__ __forceinline__ f4 mfma32(u4 a, u4 b, f4 c) { return __builtin_amdgcn_mfma_f32_16x16x32_bf16(__builtin_bit_cast(bf8, a), __builtin_bit_cast(bf8, b), c, 0, 0, 0); }
__device__ __forceinline__ u2 trread(unsigned a) { return __builtin_bit_cast(u2, __builtin_amdgcn_ds_read_tr16_b64_v4i16((LAS s4*)(size_t)a)); }
typedef __amdgpu_buffer_rsrc_t rsrc_t;
__device__ __forceinline__ rsrc_t ws_rsrc(const Params& P) { return __builtin_amdgcn_make_buffer_rsrc(P.ws, 0, OFF_DBG, 0x00020000); }
__device__ __forceinline__ u4 bld16(rsrc_t r, unsigned vo, unsigned so) { return __builtin_amdgcn_raw_buffer_load_b128(r, vo, so, 0); }
__device__ __forceinline__ u4 bld16s(rsrc_t r, unsigned vo, unsigned so) { return __builtin_amdgcn_raw_buffer_load_b128(r, vo, so, 16); }
__device__ __forceinline__ u2 bld8s(rsrc_t r, unsigned vo, unsigned so) { return __builtin_amdgcn_raw_buffer_load_b64(r, vo, so, 16); }
__device__ __forceinline__ unsigned bld4s(rsrc_t r, unsigned vo, unsigned so) { return __builtin_amdgcn_raw_buffer_load_b32(r, vo, so, 16); }
__device__ __forceinline__ void bst4s(rsrc_t r, unsigned vo, unsigned so, unsigned v) { __builtin_amdgcn_raw_buffer_store_b32(v, r, vo, so, 16); }
__device__ __forceinline__ void bst8s(rsrc_t r, unsigned vo, unsigned so, u2 v) { __builtin_amdgcn_raw_buffer_store_b64(v, r, vo, so, 16); }
__device__ __forceinline__ void bst16s(rsrc_t r, unsigned vo, unsigned so, u4 v) {
    __builtin_amdgcn_raw_buffer_store_b128(v, r, vo, so, 16);
    asm volatile("s_nop 1" :: "v"(v));
}
__device__ __forceinline__ unsigned cyc() { unsigned long long t; asm volatile("s_memtime %0\n\ts_waitcnt lgkmcnt(0)" : "=s"(t) :: "memory"); return (unsigned)t; }
#define MEMBAR() asm volatile("" ::: "memory")
#define SCHED() __builtin_amdgcn_sched_barrier(0)
#define DRAIN() asm volatile("s_waitcnt vmcnt(0)" ::: "memory")
#define PINV(x) asm volatile("" : "+v"(x))
#define PINA(x) asm volatile("" : "+a"(x))
#define LDSP(T, a) ((LAS T*)(size_t)(a))
__device__ __forceinline__ unsigned umax4(u4 v) { return max(max(v.x, v.y), max(v.z, v.w)); }
template <int CTRL> __device__ __forceinline__ unsigned dppu(unsigned v) { return (unsigned)__builtin_amdgcn_update_dpp(0, (int)v, CTRL, 0xf, 0xf, true); }
template <int CTRL> __device__ __forceinline__ float dppf(float v) { return __builtin_bit_cast(float, __builtin_amdgcn_update_dpp(0, __builtin_bit_cast(int, v), CTRL, 0xf, 0xf, true)); }
constexpr int DPP_X1 = 0xB1, DPP_X2 = 0x4E, DPP_HM = 0x141;
constexpr float LOG2E = 1.4426950408889634f;
#define F4Z ((f4){0.f, 0.f, 0.f, 0.f})

#define TL(kern, k)
#define TUN(f, c) "i"(c)
#define ROTX 0
__device__ __forceinline__ void wait_words(rsrc_t rs, unsigned vo, unsigned so, bool slow = true) {
#pragma nounroll
    for (int spin = 0; spin < SPIN_MAX; ++spin) {
        MEMBAR();
        if (!__any(bld4s(rs, vo, so) == SENT)) break;
        if (slow) __builtin_amdgcn_s_sleep(6);
    }
    MEMBAR();
}
__device__ __forceinline__ void publish(rsrc_t rs, unsigned so) {
    DRAIN();
    __syncthreads();
    if (threadIdx.x == 0) bst4s(rs, 0, so, 1u);
}

__device__ __forceinline__ u2 epoch_tag(rsrc_t rs) {
    const u2 e = bld8s(rs, 0, OFF_EP);
    const unsigned long long t = (((unsigned long long)e.y << 32) | e.x) + 1ull;
    return (u2){(unsigned)t, (unsigned)(t >> 32)};
}
__device__ __forceinline__ void wait_init(rsrc_t rs) {
    const unsigned vo = (threadIdx.x & 31u) * 8u;
#pragma nounroll
    for (int spin = 0; spin < SPIN_MAX; ++spin) {
        MEMBAR();
        const u2 v = bld8s(rs, vo, OFF_EP + 256), tag = epoch_tag(rs);
        if (!__any(v.x != tag.x || v.y != tag.y)) break;
        __builtin_amdgcn_s_sleep(2);
    }
    MEMBAR();
}

constexpr unsigned XROW = 912, XIMG = 32 * XROW;
__device__ __forceinline__ rsrc_t x_rsrc(const Params& P) { return __builtin_amdgcn_make_buffer_rsrc((void*)P.x, 0, 2048 * 896, 0x00020000); }
__device__ __forceinline__ void ximg_off(int tid, unsigned (&o)[7]) {
#pragma unroll
    for (int i = 0; i < 7; ++i) { const unsigned c = tid + 256 * i; o[i] = (c + c / 56) * 16; }
}
#define XLOAD(st, sg) _Pragma("unroll") for (int i_ = 0; i_ < 7; ++i_) st[i_] = bld16(rx, tid * 16, (sg) * 28672u + i_ * 4096u);
#define XSTORE(st, base) _Pragma("unroll") for (int i_ = 0; i_ < 7; ++i_) *LDSP(u4, (base) + xo[i_]) = st[i_];
__device__ __forceinline__ void mem_to_rows(rsrc_t rs, int s, int w, int lane, unsigned base) {
    u4 a, b;
    const unsigned so = OFF_MEMC + s * 7168 + w * 1792, v2 = min(1024u + lane * 16u, 1776u);
#pragma nounroll
    for (int spin = 0; spin < SPIN_MAX; ++spin) {
        MEMBAR();
        a = bld16s(rs, lane * 16, so); b = bld16s(rs, v2, so);
        if (!__any(max(umax4(a), umax4(b)) == SENT)) break;
        if (s < 63) __builtin_amdgcn_s_sleep(4);
    }
    const int rho = lane & 15;
    const unsigned ad = base + (2 * (rho >> 2) + (rho & 1)) * XROW + (7 * w + (lane >> 4)) * 32 + (rho & 2) * 2;
#pragma unroll
    for (int g = 0; g < 4; ++g) *LDSP(unsigned, ad + g * 8) = a[g];
    if (lane < 48) {
#pragma unroll
        for (int g = 0; g < 4; ++g) *LDSP(unsigned, ad + 128 + g * 8) = b[g];
    }
}

__device__ __forceinline__ void a_slot(int lt, int n, int& set, int& im, int& row, int& hf) {
    if (lt == 0 || lt == 1 || lt == 4 || lt == 5) { set = lt >> 2; im = 0; row = n; hf = lt & 1; }
    else { const int sg = (lt == 2 ? 0 : lt == 3 ? 16 : 32) + n; set = sg >= 24; im = 1; row = (sg - 24 * set) >> 1; hf = sg & 1; }
}
__device__ __forceinline__ int a_col(int w, int lt, int n) {
    int set, im, row, hf; a_slot(lt, n, set, im, row, hf);
    const int k2 = 16 * (row >> 2) + 8 * im + 4 * ((row >> 1) & 1) + 2 * hf + (row & 1);
    return 16 * (7 * w + (k2 >> 3)) + 4 * ((k2 & 7) >> 1) + 2 * set + (k2 & 1);
}

__device__ __forceinline__ u4 mem0_piece(const Params& P, int c) {
    const int rho = c & 15, i = 2 * (rho >> 2) + (rho & 1);
    const unsigned* mp = (const unsigned*)(P.mem0 + i * 448 + (c >> 4) * 16 + (rho & 2));
    return (u4){min(mp[0], SENT - 1), min(mp[2], SENT - 1), min(mp[4], SENT - 1), min(mp[6], SENT - 1)};
}

template <int mat>
__device__ __forceinline__ unsigned prep_gemm(const Params& P, int h, int t, unsigned lb) {
    unsigned dec;
    const int tid = threadIdx.x, lane = tid & 63, w = __builtin_amdgcn_readfirstlane(tid >> 6);
    const int n = lane & 15, q = lane >> 4;
    const rsrc_t ra = __builtin_amdgcn_make_buffer_rsrc((void*)(mat ? P.Wv : P.Wq), 0, 448 * 8192, 0x00020000);
    const rsrc_t rb = __builtin_amdgcn_make_buffer_rsrc((void*)(mat ? P.Wo : P.Wk), 0, 448 * 8192, 0x00020000);
    unsigned t0, t1;
    const bool lin = mat && w >= 2;
    const unsigned vt = lin ? h * 256 * 896 + (t * 128 + (tid & 127)) * 128 : (64 * t + ((tid & 127) >> 1)) * 8192 + h * 512 + (tid & 1) * 128;
    const rsrc_t rt = __builtin_amdgcn_make_buffer_rsrc((void*)(w < 2 ? (mat ? P.Wv : P.Wq) : (mat ? P.Wo : P.Wk)), 0, 448 * 8192, 0x00020000);
    asm volatile("buffer_load_dword %0, %1, %2, 0 offen" : "=&v"(t0) : "v"(vt), "s"(rt) : "memory");
    const unsigned v8 = (lane >> 3) * 8192 + (((lane & 7) ^ (lane >> 3)) * 16) + h * 512;
    unsigned vb[7], rdb[7];
#pragma unroll
    for (int i = 0; i < 7; ++i) {
        const int c = lane + 64 * i, r = c / 14, cc = c % 14, lr = a_col(w, i, n) - 112 * w;
        vb[i] = mat ? (h * 256 + r) * 896 + 224 * w + 16 * cc : v8 + (112 * w + 8 * i) * 8192;
        rdb[i] = lb + 8192 + w * 14336 + (mat ? (8 * q + (n >> 2)) * 224 + i * 32 + (lane & 3) * 8 : lr * 128 + ((q ^ (lr & 7)) * 16));
        PINV(vb[i]); PINV(rdb[i]);
    }
    const unsigned kq = mat ? 57344u : 128u, kh = mat ? 28672u : 56u * 8192u;
    const unsigned sa = (64 * t + 16 * w) * 8192u, la = lb + w * 2048, lbb = lb + 8192 + w * 14336;
#define DMA(ldsa, vo_, rs_, so_) asm volatile("s_mov_b32 m0, %0\n\ts_nop 0\n\tbuffer_load_dwordx4 %1, %2, %3 offen lds" :: "s"(ldsa), "v"(vo_), "s"(rs_), "s"(so_) : "memory")
#define STAGE(qt) { _Pragma("unroll") for (int j = 0; j < 2; ++j) DMA(la + ((qt) & 1) * 65536 + j * 1024, v8, ra, sa + j * 65536u + ((qt) ^ (t & 1)) * 128u); \
        _Pragma("unroll") for (int i = 0; i < 14; ++i) DMA(lbb + ((qt) & 1) * 65536 + i * 1024, vb[i % 7], rb, ((qt) ^ (t & 1)) * kq + (i / 7) * kh); }
    STAGE(0)
    asm volatile("buffer_load_dword %0, %1, %2, %3 offen" : "=&v"(t1) : "v"(vt), "s"(rt), "s"(lin ? 114688 : 256) : "memory");
    STAGE(1)
    {
        unsigned rda[2];
#pragma unroll
        for (int hf = 0; hf < 2; ++hf) { const int lr = 16 * (n >> 3) + 8 * ((n >> 2) & 1) + (n & 3) + 4 * hf; rda[hf] = lb + lr * 128 + ((q ^ (lr & 7)) * 16); }
        const rsrc_t rs = ws_rsrc(P);
        u2 iv, ie;
        u4 fr[2][22];
        auto rdf = [&](int qt, int i) {
            const unsigned bo = (qt & 1) * 65536; const int ks = i / 11, j = i % 11;
            if (j < 4) fr[qt & 1][i] = *LDSP(u4, (rda[j & 1] ^ (ks * 64)) + bo + (j >> 1) * 4096);
            else if (mat) { const u2 r0 = trread(rdb[j - 4] + bo + ks * 7168), r1 = trread(rdb[j - 4] + bo + ks * 7168 + 896); fr[qt & 1][i] = (u4){r0.x, r0.y, r1.x, r1.y}; }
            else fr[qt & 1][i] = *LDSP(u4, (rdb[j - 4] ^ (ks * 64)) + bo);
        };
        asm volatile("s_waitcnt vmcnt(17)\n\ts_barrier" :: "v"(t0) : "memory");
        TL(0, 1)
#pragma unroll
        for (int i = 0; i < 22; ++i) rdf(0, i);
        f4 acc[7][4];
#pragma unroll
        for (int i = 0; i < 7; ++i)
#pragma unroll
            for (int j = 0; j < 4; ++j) acc[i][j] = F4Z;
        const float sc = mat ? 1.0f : 0.0625f * LOG2E;
        const unsigned ob = mat ? OFF_BW + (7 * w * 16 + h) * 14336u : OFF_AW + (h * 4 + w) * 7 * 14336u;
        unsigned bo[2];
#pragma unroll
        for (int pc = 0; pc < 2; ++pc) bo[pc] = ((4 * t + 2 * pc + (q >> 1) + (mat ? 0 : 28 - 7 * w)) % 28) * 512 + (lane & 31) * 16;
        auto put = [&](int lt, int pc) {
            const f4 X = acc[lt][2 * pc] * sc, Y = acc[lt][2 * pc + 1] * sc;
            bst16s(rs, bo[pc], ob + lt * (mat ? 16u * 14336u : 14336u), (u4){pk2(X[0], X[1]), pk2(X[2], X[3]), pk2(Y[0], Y[1]), pk2(Y[2], Y[3])});
        };
#pragma unroll
        for (int qt = 0; qt < 4; ++qt) {
            if (qt < 3) asm volatile("s_waitcnt vmcnt(0) lgkmcnt(0)\n\ts_barrier" ::: "memory");
            if (qt == 0) asm volatile("" :: "v"(t1));
            if (qt == 2) { iv = bld8s(rs, (lane & 31) * 8, OFF_EP + 256); ie = bld8s(rs, 0, OFF_EP); }
            SCHED();
#pragma unroll
            for (int gI = 0; gI < 14; ++gI) {
                const int ks = qt == 3 ? gI & 1 : gI / 7, lt = qt == 3 ? gI >> 1 : gI % 7;
                if (qt < 2) {
                    if (gI < 8) {
                        const int i0 = 2 * gI;
#pragma unroll
                        for (int i = i0; i < i0 + 2; ++i) {
                            if (i < 2) DMA(la + (qt & 1) * 65536 + i * 1024, v8, ra, sa + i * 65536u + ((qt + 2) ^ (t & 1)) * 128u);
                            else DMA(lbb + (qt & 1) * 65536 + (i - 2) * 1024, vb[(i - 2) % 7], rb, ((qt + 2) ^ (t & 1)) * kq + ((i - 2) / 7) * kh);
                        }
                    }
                }
                if (qt < 3 && gI < 11) { rdf(qt + 1, 2 * gI); rdf(qt + 1, 2 * gI + 1); }
                SCHED();
#pragma unroll
                for (int j = 0; j < 4; ++j) acc[lt][j] = mfma32(fr[qt & 1][ks * 11 + j], fr[qt & 1][ks * 11 + 4 + lt], acc[lt][j]);
                if (qt == 3 && lt > 0) put(lt - 1, ks);
                SCHED();
            }
        }
        TL(0, 2)
        put(6, 0); put(6, 1);
        if (__any(iv.x != ie.x + 1u || iv.y != ie.y + (ie.x == 0xFFFFFFFFu))) wait_init(rs);
        MEMBAR();
        dec = bld4s(rs, 0, CT_ROT);
        publish(rs, CT_AB + ((mat * 16 + h) * 7 + t) * 64);
    }
    TL(0, 3)
    return __builtin_amdgcn_readfirstlane(dec);
}

__device__ __forceinline__ unsigned decide(rsrc_t rs, int lane, int gs, int gc) {
    u4 a, b;
#pragma nounroll
    for (int spin = 0; spin < SPIN_MAX; ++spin) {
        MEMBAR();
        a = bld16s(rs, (lane & 3) * 64, CT_PRB + gs * 256); b = bld16s(rs, (lane & 3) * 64, CT_PRB + gc * 256);
        if (!__any(max(umax4(a), umax4(b)) == SENT)) break;
        __builtin_amdgcn_s_sleep(2);
    }
    unsigned m = (min(min((a.x + b.x) * 4, (a.y + b.y) * 4 + 1), min((a.z + b.z) * 4 + 2, (a.w + b.w) * 4 + 3)) & 3u) << (2 * (lane & 3));
    m |= dppu<DPP_X1>(m); m |= dppu<DPP_X2>(m);
    return __builtin_amdgcn_readfirstlane(m);
}
#define GRP_S(rot) ((rot) & 7)
#define GRP_C(rot) (((rot) + 2) & 7)
__device__ __forceinline__ unsigned chunk_table(unsigned dec, int lane) {
    const int blk = lane / 5;
    return OFF_HOP + blk * 524288u + (lane - blk * 5) * 98304u + (((dec >> (2 * (blk >> 2))) ^ (blk << 1)) & 3u) * 8192u;
}

__device__ __forceinline__ unsigned phase1(const Params& P, unsigned lb) {
    const int tid = threadIdx.x, lane = tid & 63, w = __builtin_amdgcn_readfirstlane(tid >> 6), b = blockIdx.x;
    const int n = lane & 15, q = lane >> 4;
    TL(0, 0)
    const int xc = b & 7, rk = b >> 3;
    if (rk >= 28) {
        const rsrc_t rx = x_rsrc(P), rs = ws_rsrc(P);
        const int e0 = xc * 4 + rk - 28, gt = e0 * 256 + tid;
        const unsigned long long tstart = __builtin_amdgcn_s_memrealtime();
        const unsigned pb = OFF_HOP + (rk - 28) * 2097152u + 15u * 32768u + xc * 512u;
        unsigned xo[7]; ximg_off(tid, xo);
        u4 s0[7], s1[7];
        const u2 er = bld8s(rs, 0, OFF_EP);
        XLOAD(s0, 2 * e0) XLOAD(s1, 2 * e0 + 1)
        if (gt < 448) bst16s(rs, gt * 16, OFF_MEMC, mem0_piece(P, gt));
#pragma unroll
        for (unsigned i = 0; i < 4; ++i) {
            const unsigned o = gt * 16u + i * 131072u;
            const unsigned w0 = o == CT_ROT - OFF_CTRL ? (xc - __builtin_amdgcn_s_getreg(20 | (3 << 11)) + ROTX) & 7u : SENT;
            if (o < CTRL_BYTES || o >= CTRL_BYTES + 7168u) bst16s(rs, o, OFF_CTRL, (u4){w0, SENT, SENT, SENT});
        }
        if (gt >= 4096 && gt < 6656) bst16s(rs, (gt - 4096) * 16, gt < 6144 ? OFF_PX + 63u * 32768u : OFF_PM + 63u * 8192u - 32768u, (u4){SENT, SENT, SENT, SENT});
        bst4s(rs, (lane & 3) * 8192u, pb, 0u);
        SCHED();
        DRAIN();
        XSTORE(s0, lb) XSTORE(s1, lb + XIMG)
        __syncthreads();
        if (w == 0) {
            if (lane == 0) bst8s(rs, 0, OFF_EP + 256 + e0 * 8, (u2){er.x + 1u, er.y + (er.x == 0xFFFFFFFFu)});
            for (int i = 0; i < 64 && __builtin_amdgcn_s_memrealtime() - tstart < PROBE_AT; ++i) __builtin_amdgcn_s_sleep(4);
            DRAIN();
            TL(0, 6)
            unsigned l[4];
#pragma unroll
            for (int p = 0; p < 4; ++p) { const unsigned c0 = cyc(); bst4s(rs, 0, pb + 128u + p * 8192u, 0u); DRAIN(); l[p] = min(cyc() - c0, 4000u); }
#pragma unroll
            for (int rep = 0; rep < PROBE_REP; ++rep) {
                const unsigned nb = min(l[1], l[3]) < min(l[0], l[2]);
                const unsigned c0 = cyc(); bst4s(rs, 0, pb + 256u + rep * 128u + nb * 8192u, 0u); DRAIN();
                const unsigned c1 = cyc(); bst4s(rs, 0, pb + 256u + rep * 128u + (2 + nb) * 8192u, 0u); DRAIN();
                const unsigned c2 = cyc();
                if (nb) { l[1] = min(l[1], c1 - c0); l[3] = min(l[3], c2 - c1); } else { l[0] = min(l[0], c1 - c0); l[2] = min(l[2], c2 - c1); }
            }
            TL(0, 7)
            wait_init(rs);
            bst16s(rs, 0, CT_PRB + e0 * 64, (u4){l[0], l[1], l[2], l[3]});
            TL(0, 1)
        }
        __syncthreads();
        const unsigned rot = bld4s(rs, 0, CT_ROT);
#pragma unroll
        for (int i = 0; i < 16; ++i) {
            const int pc = i & 1, set = (i >> 1) & 1, jt = (i >> 2) & 1, sg = i >> 3;
            const unsigned ad = lb + sg * XIMG + (2 * n + jt) * XROW + (7 * w + min(4 * pc + q, 6)) * 32 + set * 4;
            const u4 v = {*LDSP(unsigned, ad), *LDSP(unsigned, ad + 8), *LDSP(unsigned, ad + 16), *LDSP(unsigned, ad + 24)};
            bst16s(rs, lane * 16, OFF_XK + (2 * e0 + sg) * 32768u + w * 8192u + (i & 7) * 1024u, v);
        }
        const unsigned rt = __builtin_amdgcn_readfirstlane(rot);
        const unsigned dec = decide(rs, lane, GRP_S(rt), GRP_C(rt));
        TL(0, 2)
        const unsigned ctab = chunk_table(dec, lane);
#pragma unroll
        for (int k = 0; k < 2; ++k) {
            const unsigned c = __builtin_amdgcn_readlane(ctab, 2 * e0 + k);
#pragma unroll
            for (int i = 0; i < 6; ++i) {
                u4 v = {SENT, SENT, SENT, SENT};
                if (k == 0 && i < 2 && e0 == 0 && i * 256 + tid < 448) v = mem0_piece(P, i * 256 + tid);
                bst16s(rs, tid * 16 + (i & 1) * 4096, c + (i >> 1) * 32768u, v);
            }
        }
        DRAIN();
        __syncthreads();
        if (tid == 0) bst4s(rs, 0, CT_INIT + e0 * 4, dec);
        TL(0, 3)
        return rt;
    }
    const int hh = ((xc >> 2) * 2 + (xc & 1)) * 4 + rk / 7;
    return (xc & 2) ? prep_gemm<1>(P, hh, rk % 7, lb) : prep_gemm<0>(P, hh, rk % 7, lb);
}

constexpr int LDS_BYTES = 157 * 1024;

#define WAIT_A(h) wait_words(rs, min(lane, 6) * 64, CT_AB + (h) * 448, false)
#define WAIT_B() wait_words(rs, min(lane, 27) * 64, CT_AB + 7168 + w * 1792);

#define S_ASM_TEXT \
        "s_getpc_b64 s[92:93]\n\t" \
        "Lbeg%=:\n\t" \
        "v_lshlrev_b32 v232, 3, %[l16]\n\t" \
        "s_lshl_b32 s64, %[w], 13\n\t" \
        "v_add_u32 v232, s64, v232\n\t" \
        "v_min_u32 v232, Lend%=-Lbeg%=, v232\n\t" \
        "global_load_dword v234, v232, s[92:93]\n\t" \
        "s_add_u32 s64, %[wb], 43008\n\t" \
        "buffer_load_dwordx4 v[88:91], %[l16], %[rs], s64 offen offset:0 sc1\n\t" \
        "s_add_u32 s64, %[wb], 43008\n\t" \
        "buffer_load_dwordx4 v[92:95], %[l16], %[rs], s64 offen offset:1024 sc1\n\t" \
        "s_add_u32 s64, %[wb], 45056\n\t" \
        "buffer_load_dwordx4 v[96:99], %[l16], %[rs], s64 offen offset:0 sc1\n\t" \
        "s_add_u32 s64, %[wb], 45056\n\t" \
        "buffer_load_dwordx4 v[100:103], %[l16], %[rs], s64 offen offset:1024 sc1\n\t" \
        "s_add_u32 s64, %[wb], 47104\n\t" \
        "buffer_load_dwordx4 v[104:107], %[l16], %[rs], s64 offen offset:0 sc1\n\t" \
        "s_add_u32 s64, %[wb], 47104\n\t" \
        "buffer_load_dwordx4 v[108:111], %[l16], %[rs], s64 offen offset:1024 sc1\n\t" \
        "s_add_u32 s64, %[wb], 49152\n\t" \
        "buffer_load_dwordx4 v[112:115], %[l16], %[rs], s64 offen offset:0 sc1\n\t" \
        "s_add_u32 s64, %[wb], 49152\n\t" \
        "buffer_load_dwordx4 v[116:119], %[l16], %[rs], s64 offen offset:1024 sc1\n\t" \
        "s_add_u32 s64, %[wb], 51200\n\t" \
        "buffer_load_dwordx4 v[120:123], %[l16], %[rs], s64 offen offset:0 sc1\n\t" \
        "s_add_u32 s64, %[wb], 51200\n\t" \
        "buffer_load_dwordx4 v[124:127], %[l16], %[rs], s64 offen offset:1024 sc1\n\t" \
        "s_add_u32 s64, %[wb], 53248\n\t" \
        "buffer_load_dwordx4 v[128:131], %[l16], %[rs], s64 offen offset:0 sc1\n\t" \
        "s_add_u32 s64, %[wb], 53248\n\t" \
        "buffer_load_dwordx4 v[132:135], %[l16], %[rs], s64 offen offset:1024 sc1\n\t" \
        "s_add_u32 s64, %[wb], 55296\n\t" \
        "buffer_load_dwordx4 v[136:139], %[l16], %[rs], s64 offen offset:0 sc1\n\t" \
        "s_add_u32 s64, %[wb], 55296\n\t" \
        "buffer_load_dwordx4 v[140:143], %[l16], %[rs], s64 offen offset:1024 sc1\n\t" \
        "s_add_u32 s64, %[wb], 86016\n\t" \
        "buffer_load_dwordx4 v[144:147], %[l16], %[rs], s64 offen offset:0 sc1\n\t" \
        "s_add_u32 s64, %[wb], 86016\n\t" \
        "buffer_load_dwordx4 v[148:151], %[l16], %[rs], s64 offen offset:1024 sc1\n\t" \
        "s_add_u32 s64, %[wb], 88064\n\t" \
        "buffer_load_dwordx4 v[152:155], %[l16], %[rs], s64 offen offset:0 sc1\n\t" \
        "s_add_u32 s64, %[wb], 88064\n\t" \
        "buffer_load_dwordx4 v[156:159], %[l16], %[rs], s64 offen offset:1024 sc1\n\t" \
        "s_add_u32 s64, %[wb], 0\n\t" \
        "buffer_load_dwordx4 a[0:3], %[l16], %[rs], s64 offen offset:0 sc1\n\t" \
        "s_add_u32 s64, %[wb], 0\n\t" \
        "buffer_load_dwordx4 a[4:7], %[l16], %[rs], s64 offen offset:1024 sc1\n\t" \
        "s_add_u32 s64, %[wb], 2048\n\t" \
        "buffer_load_dwordx4 a[8:11], %[l16], %[rs], s64 offen offset:0 sc1\n\t" \
        "s_add_u32 s64, %[wb], 2048\n\t" \
        "buffer_load_dwordx4 a[12:15], %[l16], %[rs], s64 offen offset:1024 sc1\n\t" \
        "s_add_u32 s64, %[wb], 4096\n\t" \
        "buffer_load_dwordx4 a[16:19], %[l16], %[rs], s64 offen offset:0 sc1\n\t" \
        "s_add_u32 s64, %[wb], 4096\n\t" \
        "buffer_load_dwordx4 a[20:23], %[l16], %[rs], s64 offen offset:1024 sc1\n\t" \
        "s_add_u32 s64, %[wb], 6144\n\t" \
        "buffer_load_dwordx4 a[24:27], %[l16], %[rs], s64 offen offset:0 sc1\n\t" \
        "s_add_u32 s64, %[wb], 6144\n\t" \
        "buffer_load_dwordx4 a[28:31], %[l16], %[rs], s64 offen offset:1024 sc1\n\t" \
        "s_add_u32 s64, %[wb], 8192\n\t" \
        "buffer_load_dwordx4 a[32:35], %[l16], %[rs], s64 offen offset:0 sc1\n\t" \
        "s_add_u32 s64, %[wb], 8192\n\t" \
        "buffer_load_dwordx4 a[36:39], %[l16], %[rs], s64 offen offset:1024 sc1\n\t" \
        "s_add_u32 s64, %[wb], 10240\n\t" \
        "buffer_load_dwordx4 a[40:43], %[l16], %[rs], s64 offen offset:0 sc1\n\t" \
        "s_add_u32 s64, %[wb], 10240\n\t" \
        "buffer_load_dwordx4 a[44:47], %[l16], %[rs], s64 offen offset:1024 sc1\n\t" \
        "s_add_u32 s64, %[wb], 12288\n\t" \
        "buffer_load_dwordx4 a[48:51], %[l16], %[rs], s64 offen offset:0 sc1\n\t" \
        "s_add_u32 s64, %[wb], 12288\n\t" \
        "buffer_load_dwordx4 a[52:55], %[l16], %[rs], s64 offen offset:1024 sc1\n\t" \
        "s_add_u32 s64, %[wb], 14336\n\t" \
        "buffer_load_dwordx4 a[56:59], %[l16], %[rs], s64 offen offset:0 sc1\n\t" \
        "s_add_u32 s64, %[wb], 14336\n\t" \
        "buffer_load_dwordx4 a[60:63], %[l16], %[rs], s64 offen offset:1024 sc1\n\t" \
        "s_add_u32 s64, %[wb], 16384\n\t" \
        "buffer_load_dwordx4 a[64:67], %[l16], %[rs], s64 offen offset:0 sc1\n\t" \
        "s_add_u32 s64, %[wb], 16384\n\t" \
        "buffer_load_dwordx4 a[68:71], %[l16], %[rs], s64 offen offset:1024 sc1\n\t" \
        "s_add_u32 s64, %[wb], 18432\n\t" \
        "buffer_load_dwordx4 a[72:75], %[l16], %[rs], s64 offen offset:0 sc1\n\t" \
        "s_add_u32 s64, %[wb], 18432\n\t" \
        "buffer_load_dwordx4 a[76:79], %[l16], %[rs], s64 offen offset:1024 sc1\n\t" \
        "s_add_u32 s64, %[wb], 20480\n\t" \
        "buffer_load_dwordx4 a[80:83], %[l16], %[rs], s64 offen offset:0 sc1\n\t" \
        "s_add_u32 s64, %[wb], 20480\n\t" \
        "buffer_load_dwordx4 a[84:87], %[l16], %[rs], s64 offen offset:1024 sc1\n\t" \
        "s_add_u32 s64, %[wb], 22528\n\t" \
        "buffer_load_dwordx4 a[88:91], %[l16], %[rs], s64 offen offset:0 sc1\n\t" \
        "s_add_u32 s64, %[wb], 22528\n\t" \
        "buffer_load_dwordx4 a[92:95], %[l16], %[rs], s64 offen offset:1024 sc1\n\t" \
        "s_add_u32 s64, %[wb], 24576\n\t" \
        "buffer_load_dwordx4 a[96:99], %[l16], %[rs], s64 offen offset:0 sc1\n\t" \
        "s_add_u32 s64, %[wb], 24576\n\t" \
        "buffer_load_dwordx4 a[100:103], %[l16], %[rs], s64 offen offset:1024 sc1\n\t" \
        "s_add_u32 s64, %[wb], 26624\n\t" \
        "buffer_load_dwordx4 a[104:107], %[l16], %[rs], s64 offen offset:0 sc1\n\t" \
        "s_add_u32 s64, %[wb], 26624\n\t" \
        "buffer_load_dwordx4 a[108:111], %[l16], %[rs], s64 offen offset:1024 sc1\n\t" \
        "s_add_u32 s64, %[wb], 28672\n\t" \
        "buffer_load_dwordx4 a[112:115], %[l16], %[rs], s64 offen offset:0 sc1\n\t" \
        "s_add_u32 s64, %[wb], 28672\n\t" \
        "buffer_load_dwordx4 a[116:119], %[l16], %[rs], s64 offen offset:1024 sc1\n\t" \
        "s_add_u32 s64, %[wb], 30720\n\t" \
        "buffer_load_dwordx4 a[120:123], %[l16], %[rs], s64 offen offset:0 sc1\n\t" \
        "s_add_u32 s64, %[wb], 30720\n\t" \
        "buffer_load_dwordx4 a[124:127], %[l16], %[rs], s64 offen offset:1024 sc1\n\t" \
        "s_add_u32 s64, %[wb], 32768\n\t" \
        "buffer_load_dwordx4 a[128:131], %[l16], %[rs], s64 offen offset:0 sc1\n\t" \
        "s_add_u32 s64, %[wb], 32768\n\t" \
        "buffer_load_dwordx4 a[132:135], %[l16], %[rs], s64 offen offset:1024 sc1\n\t" \
        "s_add_u32 s64, %[wb], 34816\n\t" \
        "buffer_load_dwordx4 a[136:139], %[l16], %[rs], s64 offen offset:0 sc1\n\t" \
        "s_add_u32 s64, %[wb], 34816\n\t" \
        "buffer_load_dwordx4 a[140:143], %[l16], %[rs], s64 offen offset:1024 sc1\n\t" \
        "s_add_u32 s64, %[wb], 36864\n\t" \
        "buffer_load_dwordx4 a[144:147], %[l16], %[rs], s64 offen offset:0 sc1\n\t" \
        "s_add_u32 s64, %[wb], 36864\n\t" \
        "buffer_load_dwordx4 a[148:151], %[l16], %[rs], s64 offen offset:1024 sc1\n\t" \
        "s_add_u32 s64, %[wb], 38912\n\t" \
        "buffer_load_dwordx4 a[152:155], %[l16], %[rs], s64 offen offset:0 sc1\n\t" \
        "s_add_u32 s64, %[wb], 38912\n\t" \
        "buffer_load_dwordx4 a[156:159], %[l16], %[rs], s64 offen offset:1024 sc1\n\t" \
        "s_add_u32 s64, %[wb], 40960\n\t" \
        "buffer_load_dwordx4 a[160:163], %[l16], %[rs], s64 offen offset:0 sc1\n\t" \
        "s_waitcnt vmcnt(58)\n\t" \
        "s_add_u32 s64, %[wb], 40960\n\t" \
        "buffer_load_dwordx4 a[164:167], %[l16], %[rs], s64 offen offset:1024 sc1\n\t" \
        "s_waitcnt vmcnt(58)\n\t" \
        "s_add_u32 s64, %[wb], 57344\n\t" \
        "buffer_load_dwordx4 a[168:171], %[l16], %[rs], s64 offen offset:0 sc1\n\t" \
        "s_waitcnt vmcnt(58)\n\t" \
        "s_add_u32 s64, %[wb], 57344\n\t" \
        "buffer_load_dwordx4 a[172:175], %[l16], %[rs], s64 offen offset:1024 sc1\n\t" \
        "s_waitcnt vmcnt(58)\n\t" \
        "s_add_u32 s64, %[wb], 59392\n\t" \
        "buffer_load_dwordx4 a[176:179], %[l16], %[rs], s64 offen offset:0 sc1\n\t" \
        "s_waitcnt vmcnt(58)\n\t" \
        "s_add_u32 s64, %[wb], 59392\n\t" \
        "buffer_load_dwordx4 a[180:183], %[l16], %[rs], s64 offen offset:1024 sc1\n\t" \
        "s_waitcnt vmcnt(58)\n\t" \
        "s_add_u32 s64, %[wb], 61440\n\t" \
        "buffer_load_dwordx4 a[184:187], %[l16], %[rs], s64 offen offset:0 sc1\n\t" \
        "s_waitcnt vmcnt(58)\n\t" \
        "s_add_u32 s64, %[wb], 61440\n\t" \
        "buffer_load_dwordx4 a[188:191], %[l16], %[rs], s64 offen offset:1024 sc1\n\t" \
        "s_waitcnt vmcnt(58)\n\t" \
        "s_add_u32 s64, %[wb], 63488\n\t" \
        "buffer_load_dwordx4 a[192:195], %[l16], %[rs], s64 offen offset:0 sc1\n\t" \
        "s_waitcnt vmcnt(58)\n\t" \
        "s_add_u32 s64, %[wb], 63488\n\t" \
        "buffer_load_dwordx4 a[196:199], %[l16], %[rs], s64 offen offset:1024 sc1\n\t" \
        "s_waitcnt vmcnt(58)\n\t" \
        "s_add_u32 s64, %[wb], 65536\n\t" \
        "buffer_load_dwordx4 a[200:203], %[l16], %[rs], s64 offen offset:0 sc1\n\t" \
        "s_waitcnt vmcnt(58)\n\t" \
        "s_add_u32 s64, %[wb], 65536\n\t" \
        "buffer_load_dwordx4 a[204:207], %[l16], %[rs], s64 offen offset:1024 sc1\n\t" \
        "s_waitcnt vmcnt(58)\n\t" \
        "s_add_u32 s64, %[wb], 67584\n\t" \
        "buffer_load_dwordx4 a[208:211], %[l16], %[rs], s64 offen offset:0 sc1\n\t" \
        "s_waitcnt vmcnt(58)\n\t" \
        "s_add_u32 s64, %[wb], 67584\n\t" \
        "buffer_load_dwordx4 a[212:215], %[l16], %[rs], s64 offen offset:1024 sc1\n\t" \
        "s_waitcnt vmcnt(58)\n\t" \
        "s_add_u32 s64, %[wb], 69632\n\t" \
        "buffer_load_dwordx4 a[216:219], %[l16], %[rs], s64 offen offset:0 sc1\n\t" \
        "s_waitcnt vmcnt(58)\n\t" \
        "s_add_u32 s64, %[wb], 69632\n\t" \
        "buffer_load_dwordx4 a[220:223], %[l16], %[rs], s64 offen offset:1024 sc1\n\t" \
        "s_waitcnt vmcnt(58)\n\t" \
        "s_add_u32 s64, %[wb], 71680\n\t" \
        "buffer_load_dwordx4 a[224:227], %[l16], %[rs], s64 offen offset:0 sc1\n\t" \
        "s_waitcnt vmcnt(58)\n\t" \
        "s_add_u32 s64, %[wb], 71680\n\t" \
        "buffer_load_dwordx4 a[228:231], %[l16], %[rs], s64 offen offset:1024 sc1\n\t" \
        "s_waitcnt vmcnt(58)\n\t" \
        "s_add_u32 s64, %[wb], 73728\n\t" \
        "buffer_load_dwordx4 a[232:235], %[l16], %[rs], s64 offen offset:0 sc1\n\t" \
        "s_waitcnt vmcnt(58)\n\t" \
        "s_add_u32 s64, %[wb], 73728\n\t" \
        "buffer_load_dwordx4 a[236:239], %[l16], %[rs], s64 offen offset:1024 sc1\n\t" \
        "s_waitcnt vmcnt(58)\n\t" \
        "s_add_u32 s64, %[wb], 75776\n\t" \
        "buffer_load_dwordx4 a[240:243], %[l16], %[rs], s64 offen offset:0 sc1\n\t" \
        "s_waitcnt vmcnt(58)\n\t" \
        "s_add_u32 s64, %[wb], 75776\n\t" \
        "buffer_load_dwordx4 a[244:247], %[l16], %[rs], s64 offen offset:1024 sc1\n\t" \
        "s_waitcnt vmcnt(58)\n\t" \
        "s_add_u32 s64, %[wb], 77824\n\t" \
        "buffer_load_dwordx4 a[248:251], %[l16], %[rs], s64 offen offset:0 sc1\n\t" \
        "s_waitcnt vmcnt(58)\n\t" \
        "s_add_u32 s64, %[wb], 77824\n\t" \
        "buffer_load_dwordx4 a[252:255], %[l16], %[rs], s64 offen offset:1024 sc1\n\t" \
        "s_waitcnt vmcnt(58)\n\t" \
        "s_add_u32 s64, %[wb], 79872\n\t" \
        "buffer_load_dwordx4 v[32:35], %[l16], %[rs], s64 offen offset:0 sc1\n\t" \
        "s_waitcnt vmcnt(58)\n\t" \
        "s_add_u32 s64, %[wb], 79872\n\t" \
        "buffer_load_dwordx4 v[36:39], %[l16], %[rs], s64 offen offset:1024 sc1\n\t" \
        "s_waitcnt vmcnt(58)\n\t" \
        "s_add_u32 s64, %[wb], 81920\n\t" \
        "buffer_load_dwordx4 v[40:43], %[l16], %[rs], s64 offen offset:0 sc1\n\t" \
        "s_waitcnt vmcnt(58)\n\t" \
        "s_add_u32 s64, %[wb], 81920\n\t" \
        "buffer_load_dwordx4 v[44:47], %[l16], %[rs], s64 offen offset:1024 sc1\n\t" \
        "s_waitcnt vmcnt(58)\n\t" \
        "s_add_u32 s64, %[wb], 83968\n\t" \
        "buffer_load_dwordx4 v[48:51], %[l16], %[rs], s64 offen offset:0 sc1\n\t" \
        "s_waitcnt vmcnt(58)\n\t" \
        "s_add_u32 s64, %[wb], 83968\n\t" \
        "buffer_load_dwordx4 v[52:55], %[l16], %[rs], s64 offen offset:1024 sc1\n\t" \
        "s_waitcnt vmcnt(58)\n\t" \
        "s_add_u32 s64, %[wb], 90112\n\t" \
        "buffer_load_dwordx4 v[192:195], %[l16], %[rs], s64 offen offset:0 sc1\n\t" \
        "s_waitcnt vmcnt(58)\n\t" \
        "s_add_u32 s64, %[wb], 90112\n\t" \
        "buffer_load_dwordx4 v[196:199], %[l16], %[rs], s64 offen offset:1024 sc1\n\t" \
        "s_waitcnt vmcnt(58)\n\t" \
        "s_add_u32 s64, %[wb], 92160\n\t" \
        "buffer_load_dwordx4 v[200:203], %[l16], %[rs], s64 offen offset:0 sc1\n\t" \
        "s_waitcnt vmcnt(58)\n\t" \
        "s_add_u32 s64, %[wb], 92160\n\t" \
        "buffer_load_dwordx4 v[204:207], %[l16], %[rs], s64 offen offset:1024 sc1\n\t" \
        "s_waitcnt vmcnt(58)\n\t" \
        "s_add_u32 s64, %[wb], 94208\n\t" \
        "buffer_load_dwordx4 v[208:211], %[l16], %[rs], s64 offen offset:0 sc1\n\t" \
        "s_waitcnt vmcnt(58)\n\t" \
        "s_add_u32 s64, %[wb], 94208\n\t" \
        "buffer_load_dwordx4 v[212:215], %[l16], %[rs], s64 offen offset:1024 sc1\n\t" \
        "s_waitcnt vmcnt(58)\n\t" \
        "s_add_u32 s64, %[wb], 96256\n\t" \
        "buffer_load_dwordx4 v[216:219], %[l16], %[rs], s64 offen offset:0 sc1\n\t" \
        "s_waitcnt vmcnt(58)\n\t" \
        "s_add_u32 s64, %[wb], 96256\n\t" \
        "buffer_load_dwordx4 v[220:223], %[l16], %[rs], s64 offen offset:1024 sc1\n\t" \
        "s_waitcnt vmcnt(58)\n\t" \
        "s_add_u32 s64, %[wb], 98304\n\t" \
        "buffer_load_dwordx4 v[224:227], %[l16], %[rs], s64 offen offset:0 sc1\n\t" \
        "s_waitcnt vmcnt(58)\n\t" \
        "s_add_u32 s64, %[wb], 98304\n\t" \
        "buffer_load_dwordx4 v[228:231], %[l16], %[rs], s64 offen offset:1024 sc1\n\t" \
        "s_waitcnt vmcnt(58)\n\t" \
        "ds_write_b128 %[lw], v[88:91] offset:0\n\t" \
        "ds_write_b128 %[lw], v[92:95] offset:1024\n\t" \
        "ds_write_b128 %[lw], v[96:99] offset:2048\n\t" \
        "ds_write_b128 %[lw], v[100:103] offset:3072\n\t" \
        "ds_write_b128 %[lw], v[104:107] offset:4096\n\t" \
        "ds_write_b128 %[lw], v[108:111] offset:5120\n\t" \
        "ds_write_b128 %[lw], v[112:115] offset:6144\n\t" \
        "ds_write_b128 %[lw], v[116:119] offset:7168\n\t" \
        "ds_write_b128 %[lw], v[120:123] offset:8192\n\t" \
        "ds_write_b128 %[lw], v[124:127] offset:9216\n\t" \
        "ds_write_b128 %[lw], v[128:131] offset:10240\n\t" \
        "ds_write_b128 %[lw], v[132:135] offset:11264\n\t" \
        "ds_write_b128 %[lw], v[136:139] offset:12288\n\t" \
        "ds_write_b128 %[lw], v[140:143] offset:13312\n\t" \
        "ds_write_b128 %[lw], v[144:147] offset:14336\n\t" \
        "ds_write_b128 %[lw], v[148:151] offset:15360\n\t" \
        "ds_write_b128 %[lw], v[152:155] offset:16384\n\t" \
        "ds_write_b128 %[lw], v[156:159] offset:17408\n\t" \
        "s_lshl_b32 s90, %[w], 13\n\t" \
        "s_add_u32 s90, s90, %[oxk]\n\t" \
        "s_mov_b32 s68, s90\n\t" \
        "s_add_u32 s69, s68, 4096\n\t" \
        "v_lshrrev_b32 v232, 2, %[l16]\n\t" \
        "v_and_b32 v233, 0x7c, v232\n\t" \
        "v_mov_b32 v251, %[tb]\n\t" \
        "s_movk_i32 s65, 16384\n\t" \
        "s_mov_b32 s64, %[opr]\n\t" \
        "Lpg%=:\n\t" \
        "buffer_load_dword v236, v233, %[rs], s64 offen sc1\n\t" \
        "s_waitcnt vmcnt(0)\n\t" \
        "v_cmp_eq_u32 vcc, -1, v236\n\t" \
        "s_cbranch_vccz Lpgd%=\n\t" \
        "s_sub_u32 s65, s65, 1\n\t" \
        "s_cbranch_scc0 Lpg%=\n\t" \
        "Lpgd%=:\n\t" \
        "v_lshrrev_b32 v236, %[tsh], v236\n\t" \
        "v_and_b32 v236, 3, v236\n\t" \
        "v_lshlrev_b32 v236, 13, v236\n\t" \
        "v_xor_b32 v251, %[tb], v236\n\t" \
        "buffer_load_dwordx4 v[56:59], %[l16], %[rs], s68 offen offset:0 sc1\n\t" \
        "buffer_load_dwordx4 v[60:63], %[l16], %[rs], s68 offen offset:1024 sc1\n\t" \
        "buffer_load_dwordx4 v[64:67], %[l16], %[rs], s68 offen offset:2048 sc1\n\t" \
        "buffer_load_dwordx4 v[68:71], %[l16], %[rs], s68 offen offset:3072 sc1\n\t" \
        "buffer_load_dwordx4 v[72:75], %[l16], %[rs], s69 offen offset:0 sc1\n\t" \
        "buffer_load_dwordx4 v[76:79], %[l16], %[rs], s69 offen offset:1024 sc1\n\t" \
        "buffer_load_dwordx4 v[80:83], %[l16], %[rs], s69 offen offset:2048 sc1\n\t" \
        "buffer_load_dwordx4 v[84:87], %[l16], %[rs], s69 offen offset:3072 sc1\n\t" \
        "s_memtime s[40:41]\n\t" \
        "s_memrealtime s[42:43]\n\t" \
        "s_nop 2\n\t" \
        "s_memrealtime s[44:45]\n\t" \
        "s_nop 2\n\t" \
        "s_memrealtime s[46:47]\n\t" \
        "s_nop 2\n\t" \
        "s_memrealtime s[48:49]\n\t" \
        "s_nop 2\n\t" \
        "s_memrealtime s[50:51]\n\t" \
        "s_nop 2\n\t" \
        "s_memrealtime s[52:53]\n\t" \
        "s_nop 2\n\t" \
        "s_memrealtime s[54:55]\n\t" \
        "s_nop 2\n\t" \
        "s_memrealtime s[56:57]\n\t" \
        "s_waitcnt lgkmcnt(0)\n\t" \
        "s_mov_b32 s41, 0\n\t" \
        "s_cmp_eq_u32 s44, s42\n\t" \
        "s_addc_u32 s41, s41, 0\n\t" \
        "s_cmp_eq_u32 s46, s42\n\t" \
        "s_addc_u32 s41, s41, 0\n\t" \
        "s_cmp_eq_u32 s48, s42\n\t" \
        "s_addc_u32 s41, s41, 0\n\t" \
        "s_cmp_eq_u32 s50, s42\n\t" \
        "s_addc_u32 s41, s41, 0\n\t" \
        "s_cmp_eq_u32 s52, s42\n\t" \
        "s_addc_u32 s41, s41, 0\n\t" \
        "s_cmp_eq_u32 s54, s42\n\t" \
        "s_addc_u32 s41, s41, 0\n\t" \
        "s_cmp_eq_u32 s56, s42\n\t" \
        "s_addc_u32 s41, s41, 0\n\t" \
        "s_lshl_b32 s41, s41, 4\n\t" \
        "s_add_u32 s40, s40, s41\n\t" \
        "s_mul_i32 s41, s42, 24\n\t" \
        "s_sub_u32 s40, s40, s41\n\t" \
        "s_sub_u32 s40, s40, 84\n\t" \
        "s_mov_b32 s97, s40\n\t" \
        "s_mov_b32 s63, s40\n\t" \
        "ds_read_b128 v[116:119], %[lw] offset:0\n\t" \
        "ds_read_b128 v[120:123], %[lw] offset:1024\n\t" \
        "ds_read_b128 v[124:127], %[lw] offset:2048\n\t" \
        "ds_read_b128 v[128:131], %[lw] offset:3072\n\t" \
        "ds_read_b128 v[132:135], %[lw] offset:4096\n\t" \
        "ds_read_b128 v[136:139], %[lw] offset:5120\n\t" \
        "ds_read_b128 v[140:143], %[lw] offset:6144\n\t" \
        "ds_read_b128 v[144:147], %[lw] offset:7168\n\t" \
        "v_mov_b32 v148, 0\n\t" \
        "v_mov_b32 v149, 0\n\t" \
        "v_mov_b32 v150, 0\n\t" \
        "v_mov_b32 v151, 0\n\t" \
        "v_mov_b32 v152, 0\n\t" \
        "v_mov_b32 v153, 0\n\t" \
        "v_mov_b32 v154, 0\n\t" \
        "v_mov_b32 v155, 0\n\t" \
        "v_mov_b32 v156, 0\n\t" \
        "v_mov_b32 v157, 0\n\t" \
        "v_mov_b32 v158, 0\n\t" \
        "v_mov_b32 v159, 0\n\t" \
        "v_mov_b32 v160, 0\n\t" \
        "v_mov_b32 v161, 0\n\t" \
        "v_mov_b32 v162, 0\n\t" \
        "v_mov_b32 v163, 0\n\t" \
        "v_mov_b32 v164, 0\n\t" \
        "v_mov_b32 v165, 0\n\t" \
        "v_mov_b32 v166, 0\n\t" \
        "v_mov_b32 v167, 0\n\t" \
        "v_mov_b32 v168, 0\n\t" \
        "v_mov_b32 v169, 0\n\t" \
        "v_mov_b32 v170, 0\n\t" \
        "v_mov_b32 v171, 0\n\t" \
        "v_mov_b32 v172, 0\n\t" \
        "v_mov_b32 v173, 0\n\t" \
        "v_mov_b32 v174, 0\n\t" \
        "v_mov_b32 v175, 0\n\t" \
        "v_mov_b32 v176, 0\n\t" \
        "v_mov_b32 v177, 0\n\t" \
        "v_mov_b32 v178, 0\n\t" \
        "v_mov_b32 v179, 0\n\t" \
        "v_mov_b32 v180, 0\n\t" \
        "v_mov_b32 v181, 0\n\t" \
        "v_mov_b32 v182, 0\n\t" \
        "v_mov_b32 v183, 0\n\t" \
        "v_mov_b32 v184, 0\n\t" \
        "v_mov_b32 v185, 0\n\t" \
        "v_mov_b32 v186, 0\n\t" \
        "v_mov_b32 v187, 0\n\t" \
        "v_mov_b32 v188, 0\n\t" \
        "v_mov_b32 v189, 0\n\t" \
        "v_mov_b32 v190, 0\n\t" \
        "v_mov_b32 v191, 0\n\t" \
        "s_waitcnt lgkmcnt(0)\n\t" \
        "s_barrier\n\t" \
        "s_memtime s[92:93]\n\t" \
        "s_waitcnt lgkmcnt(0)\n\t" \
        "s_sub_u32 s62, s92, s63\n\t" \
        "s_add_u32 s62, s62, %[l0]\n\t" \
        "s_mov_b32 s60, 0\n\t" \
        "s_movk_i32 s98, 2250\n\t" \
        "v_readlane_b32 s99, v251, 0\n\t" \
        "s_min_u32 s64, s62, -2\n\t" \
        "v_mov_b32 v232, s64\n\t" \
        "v_mov_b32 v233, %[ks0]\n\t" \
        "s_add_u32 s73, s99, %[so]\n\t" \
        "s_nop 3\n\t" \
        "buffer_store_dwordx2 v[232:233], off, %[rs], s73 sc1\n\t" \
        "s_add_u32 s91, %[w], 2\n\t" \
        "s_and_b32 s91, s91, 3\n\t" \
        "s_mul_i32 s91, s91, 16\n\t" \
        "s_cmp_eq_u32 %[w], 0\n\t" \
        "s_cbranch_scc1 Ls0%=\n\t" \
        "s_cmp_eq_u32 %[w], 1\n\t" \
        "s_cbranch_scc1 Ls1%=\n\t" \
        "s_branch Ls2%=\n\t" \
        "Ls0%=:\n\t" \
        "v_readlane_b32 s61, v251, s60\n\t" \
        "s_mov_b32 s66, 0\n\t" \
        "s_add_u32 s72, s60, 1\n\t" \
        "Lw0%=:\n\t" \
        "s_add_u32 s73, s62, s91\n\t" \
        "s_movk_i32 s65, 4096\n\t" \
        "s_add_u32 s94, s73, s97\n\t" \
        "1:\n\t" \
        "s_memtime s[92:93]\n\t" \
        "s_sub_u32 s65, s65, 1\n\t" \
        "s_cbranch_scc1 3f\n\t" \
        "s_waitcnt lgkmcnt(0)\n\t" \
        "s_sub_u32 s64, s94, s92\n\t" \
        "s_cmp_gt_i32 s64, 384\n\t" \
        "s_cbranch_scc1 1b\n\t" \
        "s_cmp_lt_i32 s64, -300\n\t" \
        "s_cbranch_scc1 Lp0%=\n\t" \
        "s_sub_i32 s64, s64, 136\n\t" \
        "s_max_i32 s64, s64, 0\n\t" \
        "s_and_b32 s64, s64, 0xfc\n\t" \
        "s_getpc_b64 s[92:93]\n\t" \
        "2:\n\t" \
        "s_sub_u32 s64, 3f-2b, s64\n\t" \
        "s_add_u32 s92, s92, s64\n\t" \
        "s_addc_u32 s93, s93, 0\n\t" \
        "s_setpc_b64 s[92:93]\n\t" \
        "s_nop 0\n\t" \
        "s_nop 0\n\t" \
        "s_nop 0\n\t" \
        "s_nop 0\n\t" \
        "s_nop 0\n\t" \
        "s_nop 0\n\t" \
        "s_nop 0\n\t" \
        "s_nop 0\n\t" \
        "s_nop 0\n\t" \
        "s_nop 0\n\t" \
        "s_nop 0\n\t" \
        "s_nop 0\n\t" \
        "s_nop 0\n\t" \
        "s_nop 0\n\t" \
        "s_nop 0\n\t" \
        "s_nop 0\n\t" \
        "s_nop 0\n\t" \
        "s_nop 0\n\t" \
        "s_nop 0\n\t" \
        "s_nop 0\n\t" \
        "s_nop 0\n\t" \
        "s_nop 0\n\t" \
        "s_nop 0\n\t" \
        "s_nop 0\n\t" \
        "s_nop 0\n\t" \
        "s_nop 0\n\t" \
        "s_nop 0\n\t" \
        "s_nop 0\n\t" \
        "s_nop 0\n\t" \
        "s_nop 0\n\t" \
        "s_nop 0\n\t" \
        "s_nop 0\n\t" \
        "s_nop 0\n\t" \
        "s_nop 0\n\t" \
        "s_nop 0\n\t" \
        "s_nop 0\n\t" \
        "s_nop 0\n\t" \
        "s_nop 0\n\t" \
        "s_nop 0\n\t" \
        "s_nop 0\n\t" \
        "s_nop 0\n\t" \
        "s_nop 0\n\t" \
        "s_nop 0\n\t" \
        "s_nop 0\n\t" \
        "s_nop 0\n\t" \
        "s_nop 0\n\t" \
        "s_nop 0\n\t" \
        "s_nop 0\n\t" \
        "s_nop 0\n\t" \
        "s_nop 0\n\t" \
        "s_nop 0\n\t" \
        "s_nop 0\n\t" \
        "s_nop 0\n\t" \
        "s_nop 0\n\t" \
        "s_nop 0\n\t" \
        "s_nop 0\n\t" \
        "s_nop 0\n\t" \
        "s_nop 0\n\t" \
        "s_nop 0\n\t" \
        "s_nop 0\n\t" \
        "s_nop 0\n\t" \
        "s_nop 0\n\t" \
        "s_nop 0\n\t" \
        "s_nop 0\n\t" \
        "3:\n\t" \
        "s_waitcnt lgkmcnt(0)\n\t" \
        "Ll0%=:\n\t" \
        "buffer_load_dwordx4 v[88:91], %[m0], %[rs], s61 offen sc1\n\t" \
        "s_nop 13\n\t" \
        "buffer_load_dwordx4 v[92:95], %[m1], %[rs], s61 offen sc1\n\t" \
        "s_nop 13\n\t" \
        "buffer_load_dwordx4 v[96:99], %[m2], %[rs], s61 offen sc1\n\t" \
        "s_nop 13\n\t" \
        "buffer_load_dwordx4 v[100:103], %[m3], %[rs], s61 offen sc1\n\t" \
        "s_nop 13\n\t" \
        "buffer_load_dwordx4 v[104:107], %[m4], %[rs], s61 offen sc1\n\t" \
        "s_nop 13\n\t" \
        "buffer_load_dwordx4 v[108:111], %[m5], %[rs], s61 offen sc1\n\t" \
        "s_nop 13\n\t" \
        "buffer_load_dwordx4 v[112:115], %[m6], %[rs], s61 offen sc1\n\t" \
        "Lm0%=:\n\t" \
        "s_waitcnt vmcnt(6)\n\t" \
        "v_smfmac_f32_16x16x64_bf16 v[160:163], v[88:91], v[116:123], %[idx]\n\t" \
        "ds_read_b128 v[116:119], %[lw] offset:8192\n\t" \
        "v_smfmac_f32_16x16x64_bf16 v[148:151], v[88:91], a[0:7], %[idx]\n\t" \
        "ds_read_b128 v[120:123], %[lw] offset:9216\n\t" \
        "v_smfmac_f32_16x16x64_bf16 v[152:155], v[88:91], a[56:63], %[idx]\n\t" \
        "v_smfmac_f32_16x16x64_bf16 v[156:159], v[88:91], a[112:119], %[idx]\n\t" \
        "s_waitcnt vmcnt(5)\n\t" \
        "v_smfmac_f32_16x16x64_bf16 v[160:163], v[92:95], v[124:131], %[idx]\n\t" \
        "ds_read_b128 v[124:127], %[lw] offset:10240\n\t" \
        "v_smfmac_f32_16x16x64_bf16 v[148:151], v[92:95], a[8:15], %[idx]\n\t" \
        "ds_read_b128 v[128:131], %[lw] offset:11264\n\t" \
        "v_smfmac_f32_16x16x64_bf16 v[152:155], v[92:95], a[64:71], %[idx]\n\t" \
        "v_smfmac_f32_16x16x64_bf16 v[156:159], v[92:95], a[120:127], %[idx]\n\t" \
        "s_waitcnt vmcnt(4)\n\t" \
        "v_smfmac_f32_16x16x64_bf16 v[160:163], v[96:99], v[132:139], %[idx]\n\t" \
        "ds_read_b128 v[132:135], %[lw] offset:12288\n\t" \
        "v_smfmac_f32_16x16x64_bf16 v[148:151], v[96:99], a[16:23], %[idx]\n\t" \
        "ds_read_b128 v[136:139], %[lw] offset:13312\n\t" \
        "v_smfmac_f32_16x16x64_bf16 v[152:155], v[96:99], a[72:79], %[idx]\n\t" \
        "v_smfmac_f32_16x16x64_bf16 v[156:159], v[96:99], a[128:135], %[idx]\n\t" \
        "v_cmp_u_f32 s[70:71], v160, v162\n\t" \
        "s_waitcnt vmcnt(3)\n\t" \
        "v_smfmac_f32_16x16x64_bf16 v[160:163], v[100:103], v[140:147], %[idx]\n\t" \
        "ds_read_b128 v[140:143], %[lw] offset:14336\n\t" \
        "v_smfmac_f32_16x16x64_bf16 v[148:151], v[100:103], a[24:31], %[idx]\n\t" \
        "ds_read_b128 v[144:147], %[lw] offset:15360\n\t" \
        "v_smfmac_f32_16x16x64_bf16 v[152:155], v[100:103], a[80:87], %[idx]\n\t" \
        "s_cmp_lg_u64 s[70:71], 0\n\t" \
        "s_cbranch_scc1 Le0%=\n\t" \
        "Lf0%=:\n\t" \
        "v_smfmac_f32_16x16x64_bf16 v[156:159], v[100:103], a[136:143], %[idx]\n\t" \
        "s_waitcnt vmcnt(2) lgkmcnt(6)\n\t" \
        "v_smfmac_f32_16x16x64_bf16 v[160:163], v[104:107], v[116:123], %[idx]\n\t" \
        "ds_read_b128 v[116:119], %[lw] offset:16384\n\t" \
        "v_smfmac_f32_16x16x64_bf16 v[148:151], v[104:107], a[32:39], %[idx]\n\t" \
        "ds_read_b128 v[120:123], %[lw] offset:17408\n\t" \
        "v_smfmac_f32_16x16x64_bf16 v[152:155], v[104:107], a[88:95], %[idx]\n\t" \
        "v_smfmac_f32_16x16x64_bf16 v[156:159], v[104:107], a[144:151], %[idx]\n\t" \
        "s_waitcnt vmcnt(1) lgkmcnt(6)\n\t" \
        "v_smfmac_f32_16x16x64_bf16 v[160:163], v[108:111], v[124:131], %[idx]\n\t" \
        "ds_read_b128 v[124:127], %[lw] offset:2048\n\t" \
        "v_smfmac_f32_16x16x64_bf16 v[148:151], v[108:111], a[40:47], %[idx]\n\t" \
        "ds_read_b128 v[128:131], %[lw] offset:3072\n\t" \
        "v_smfmac_f32_16x16x64_bf16 v[152:155], v[108:111], a[96:103], %[idx]\n\t" \
        "v_smfmac_f32_16x16x64_bf16 v[156:159], v[108:111], a[152:159], %[idx]\n\t" \
        "s_waitcnt vmcnt(0) lgkmcnt(6)\n\t" \
        "v_smfmac_f32_16x16x64_bf16 v[160:163], v[112:115], v[132:139], %[idx]\n\t" \
        "ds_read_b128 v[132:135], %[lw] offset:4096\n\t" \
        "v_smfmac_f32_16x16x64_bf16 v[148:151], v[112:115], a[48:55], %[idx]\n\t" \
        "ds_read_b128 v[136:139], %[lw] offset:5120\n\t" \
        "v_smfmac_f32_16x16x64_bf16 v[152:155], v[112:115], a[104:111], %[idx]\n\t" \
        "v_smfmac_f32_16x16x64_bf16 v[156:159], v[112:115], a[160:167], %[idx]\n\t" \
        "s_waitcnt lgkmcnt(4)\n\t" \
        "v_add_f32 v232, v160, v162\n\t" \
        "v_smfmac_f32_16x16x64_bf16 v[172:175], v[88:91], v[140:147], %[idx]\n\t" \
        "ds_read_b128 v[140:143], %[lw] offset:6144\n\t" \
        "v_add_f32 v233, v161, v163\n\t" \
        "v_smfmac_f32_16x16x64_bf16 v[164:167], v[88:91], a[168:175], %[idx]\n\t" \
        "ds_read_b128 v[144:147], %[lw] offset:7168\n\t" \
        "v_cmp_u_f32 s[70:71], v232, v233\n\t" \
        "v_smfmac_f32_16x16x64_bf16 v[172:175], v[92:95], v[116:123], %[idx]\n\t" \
        "ds_read_b128 v[116:119], %[lw] offset:0\n\t" \
        "v_cvt_pk_bf16_f32 v237, v232, v233\n\t" \
        "v_smfmac_f32_16x16x64_bf16 v[164:167], v[92:95], a[176:183], %[idx]\n\t" \
        "ds_read_b128 v[120:123], %[lw] offset:1024\n\t" \
        "v_add_f32 v232, v148, v150\n\t" \
        "v_smfmac_f32_16x16x64_bf16 v[172:175], v[96:99], v[192:199], %[idx]\n\t" \
        "s_cmp_lg_u64 s[70:71], 0\n\t" \
        "s_cbranch_scc1 Ln0%=\n\t" \
        "Lc0%=:\n\t" \
        "v_add_f32 v233, v149, v151\n\t" \
        "v_smfmac_f32_16x16x64_bf16 v[164:167], v[96:99], a[184:191], %[idx]\n\t" \
        "ds_write_b32 %[wl1], v237\n\t" \
        "v_cvt_pk_bf16_f32 v234, v232, v233\n\t" \
        "v_smfmac_f32_16x16x64_bf16 v[172:175], v[100:103], v[200:207], %[idx]\n\t" \
        "v_add_f32 v232, v152, v154\n\t" \
        "v_smfmac_f32_16x16x64_bf16 v[164:167], v[100:103], a[192:199], %[idx]\n\t" \
        "v_add_f32 v233, v153, v155\n\t" \
        "v_smfmac_f32_16x16x64_bf16 v[172:175], v[104:107], v[208:215], %[idx]\n\t" \
        "v_cvt_pk_bf16_f32 v235, v232, v233\n\t" \
        "v_smfmac_f32_16x16x64_bf16 v[164:167], v[104:107], a[200:207], %[idx]\n\t" \
        "ds_write_b64 %[awr], v[234:235]\n\t" \
        "v_smfmac_f32_16x16x64_bf16 v[172:175], v[108:111], v[216:223], %[idx]\n\t" \
        "ds_read_b64_tr_b16 v[240:241], %[atr]\n\t" \
        "v_add_f32 v232, v156, v158\n\t" \
        "v_smfmac_f32_16x16x64_bf16 v[164:167], v[108:111], a[208:215], %[idx]\n\t" \
        "v_add_f32 v233, v157, v159\n\t" \
        "v_smfmac_f32_16x16x64_bf16 v[172:175], v[112:115], v[224:231], %[idx]\n\t" \
        "v_cvt_pk_bf16_f32 v236, v232, v233\n\t" \
        "v_smfmac_f32_16x16x64_bf16 v[164:167], v[112:115], a[216:223], %[idx]\n\t" \
        "ds_write_b32 %[wl0], v236\n\t" \
        "v_smfmac_f32_16x16x64_bf16 v[168:171], v[88:91], a[224:231], %[idx]\n\t" \
        "ds_read_b64_tr_b16 v[242:243], %[atr] offset:640\n\t" \
        "v_add_f32 v232, v172, v174\n\t" \
        "v_smfmac_f32_16x16x64_bf16 v[168:171], v[92:95], a[232:239], %[idx]\n\t" \
        "v_add_f32 v233, v173, v175\n\t" \
        "v_smfmac_f32_16x16x64_bf16 v[168:171], v[96:99], a[240:247], %[idx]\n\t" \
        "v_cvt_pk_bf16_f32 v238, v232, v233\n\t" \
        "v_smfmac_f32_16x16x64_bf16 v[168:171], v[100:103], a[248:255], %[idx]\n\t" \
        "ds_write_b32 %[wl2], v238\n\t" \
        "v_smfmac_f32_16x16x64_bf16 v[168:171], v[104:107], v[32:39], %[idx]\n\t" \
        "ds_read_b64_tr_b16 v[246:247], %[atr] offset:1920\n\t" \
        "v_add_f32 v232, v164, v166\n\t" \
        "v_smfmac_f32_16x16x64_bf16 v[168:171], v[108:111], v[40:47], %[idx]\n\t" \
        "v_add_f32 v233, v165, v167\n\t" \
        "v_smfmac_f32_16x16x64_bf16 v[168:171], v[112:115], v[48:55], %[idx]\n\t" \
        "s_waitcnt lgkmcnt(2)\n\t" \
        "v_cvt_pk_bf16_f32 v234, v232, v233\n\t" \
        "v_smfmac_f32_16x16x64_bf16 v[184:187], v[240:243], v[88:95], %[idx]\n\t" \
        "v_mov_b64 v[148:149], 0\n\t" \
        "v_smfmac_f32_16x16x64_bf16 v[176:179], v[240:243], v[56:63], %[idx]\n\t" \
        "s_nop 0\n\t" \
        "v_pk_add_f32 v[232:233], v[168:169], v[170:171]\n\t" \
        "v_cvt_pk_bf16_f32 v235, v232, v233\n\t" \
        "ds_write_b64 %[awr], v[234:235] offset:1280\n\t" \
        "ds_read_b64_tr_b16 v[244:245], %[atr] offset:1280\n\t" \
        "v_smfmac_f32_16x16x64_bf16 v[180:183], v[240:243], v[72:79], %[idx]\n\t" \
        "v_add_f32 v238, v184, v186\n\t" \
        "v_add_f32 v239, v185, v187\n\t" \
        "v_mov_b64 v[150:151], 0\n\t" \
        "v_mov_b64 v[152:153], 0\n\t" \
        "v_mov_b64 v[154:155], 0\n\t" \
        "v_mov_b64 v[156:157], 0\n\t" \
        "v_mov_b64 v[158:159], 0\n\t" \
        "v_mov_b64 v[160:161], 0\n\t" \
        "v_mov_b64 v[162:163], 0\n\t" \
        "v_mov_b64 v[164:165], 0\n\t" \
        "v_mov_b64 v[166:167], 0\n\t" \
        "v_mov_b64 v[168:169], 0\n\t" \
        "v_mov_b64 v[170:171], 0\n\t" \
        "s_waitcnt lgkmcnt(0)\n\t" \
        "v_smfmac_f32_16x16x64_bf16 v[188:191], v[244:247], v[88:95], %[idx]\n\t" \
        "v_smfmac_f32_16x16x64_bf16 v[176:179], v[244:247], v[64:71], %[idx]\n\t" \
        "v_smfmac_f32_16x16x64_bf16 v[180:183], v[244:247], v[80:87], %[idx]\n\t" \
        "s_nop 0\n\t" \
        "v_add_f32 v254, v189, v191\n\t" \
        "v_cndmask_b32_e64 v254, v239, v254, %[hm]\n\t" \
        "v_add_f32 v250, v188, v190\n\t" \
        "v_cndmask_b32_e64 v250, v238, v250, %[hm]\n\t" \
        "v_add_f32_dpp v254, v254, v254 quad_perm:[2,3,0,1] row_mask:0xf bank_mask:0xf\n\t" \
        "v_add_f32 v252, v177, v179\n\t" \
        "v_add_f32_dpp v250, v250, v250 quad_perm:[2,3,0,1] row_mask:0xf bank_mask:0xf\n\t" \
        "v_add_f32 v248, v176, v178\n\t" \
        "v_add_f32 v253, v181, v183\n\t" \
        "ds_write_b96 %[ax], v[252:254] offset:3072\n\t" \
        "v_add_f32 v249, v180, v182\n\t" \
        "v_mov_b64 v[172:173], 0\n\t" \
        "v_mov_b64 v[174:175], 0\n\t" \
        "s_waitcnt lgkmcnt(0)\n\t" \
        "s_barrier\n\t" \
        "ds_read_b128 v[96:99], %[ax] offset:0\n\t" \
        "ds_read_b128 v[100:103], %[ax] offset:1024\n\t" \
        "ds_read_b128 v[104:107], %[ax] offset:2048\n\t" \
        "v_mov_b64 v[176:177], 0\n\t" \
        "v_mov_b64 v[178:179], 0\n\t" \
        "v_mov_b64 v[180:181], 0\n\t" \
        "v_mov_b64 v[182:183], 0\n\t" \
        "v_mov_b64 v[184:185], 0\n\t" \
        "v_mov_b64 v[186:187], 0\n\t" \
        "v_mov_b64 v[188:189], 0\n\t" \
        "v_mov_b64 v[190:191], 0\n\t" \
        "s_waitcnt lgkmcnt(2)\n\t" \
        "v_add_f32 v250, v250, v98\n\t" \
        "v_pk_add_f32 v[248:249], v[248:249], v[96:97]\n\t" \
        "s_waitcnt lgkmcnt(1)\n\t" \
        "v_add_f32 v250, v250, v102\n\t" \
        "v_pk_add_f32 v[248:249], v[248:249], v[100:101]\n\t" \
        "s_waitcnt lgkmcnt(0)\n\t" \
        "v_add_f32 v250, v250, v106\n\t" \
        "v_pk_add_f32 v[248:249], v[248:249], v[104:105]\n\t" \
        "v_max3_f32 v232, v248, v249, v250\n\t" \
        "s_nop 1\n\t" \
        "v_max_f32_dpp v233, v232, v232 quad_perm:[1,0,3,2] row_mask:0xf bank_mask:0xf\n\t" \
        "v_max_f32_dpp v233, v232, v233 quad_perm:[2,3,0,1] row_mask:0xf bank_mask:0xf\n\t" \
        "v_max_f32_dpp v233, v232, v233 quad_perm:[3,2,1,0] row_mask:0xf bank_mask:0xf\n\t" \
        "s_nop 1\n\t" \
        "v_max_f32_dpp v232, v233, v233 row_half_mirror row_mask:0xf bank_mask:0xf\n\t" \
        "v_max_f32_dpp v232, v233, v232 row_mirror row_mask:0xf bank_mask:0xf\n\t" \
        "v_max_f32_dpp v232, v233, v232 row_ror:8 row_mask:0xf bank_mask:0xf\n\t" \
        "v_sub_f32 v250, v250, v232\n\t" \
        "v_pk_add_f32 v[248:249], v[248:249], v[232:233] op_sel_hi:[1,0] neg_lo:[0,1] neg_hi:[0,1]\n\t" \
        "v_exp_f32 v250, v250\n\t" \
        "v_exp_f32 v248, v248\n\t" \
        "v_exp_f32 v249, v249\n\t" \
        "v_fma_f32 v232, v250, 0.5, v248\n\t" \
        "v_add_f32 v232, v232, v249\n\t" \
        "v_mov_b32_dpp v238, v250 quad_perm:[0,0,0,0] row_mask:0xf bank_mask:0xf\n\t" \
        "v_mov_b32_dpp v239, v250 quad_perm:[1,1,1,1] row_mask:0xf bank_mask:0xf\n\t" \
        "v_add_f32_dpp v233, v232, v232 quad_perm:[1,0,3,2] row_mask:0xf bank_mask:0xf\n\t" \
        "v_add_f32_dpp v233, v232, v233 quad_perm:[2,3,0,1] row_mask:0xf bank_mask:0xf\n\t" \
        "v_add_f32_dpp v233, v232, v233 quad_perm:[3,2,1,0] row_mask:0xf bank_mask:0xf\n\t" \
        "s_nop 1\n\t" \
        "v_add_f32_dpp v232, v233, v233 row_half_mirror row_mask:0xf bank_mask:0xf\n\t" \
        "v_add_f32_dpp v232, v233, v232 row_mirror row_mask:0xf bank_mask:0xf\n\t" \
        "v_add_f32_dpp v232, v233, v232 row_ror:8 row_mask:0xf bank_mask:0xf\n\t" \
        "v_rcp_f32 v232, v232\n\t" \
        "s_nop 0\n\t" \
        "v_pk_mul_f32 v[248:249], v[248:249], v[232:233] op_sel_hi:[1,0]\n\t" \
        "v_cvt_pk_bf16_f32 v248, |v248|, |v249|\n\t" \
        "buffer_store_dword v248, %[vpx], %[rs], s61 offen sc1\n\t" \
        "v_pk_mul_f32 v[238:239], v[238:239], v[232:233] op_sel_hi:[1,0]\n\t" \
        "v_cvt_pk_bf16_f32 v238, |v238|, |v239|\n\t" \
        "buffer_store_dword v238, %[vpm], %[rs], s61 offen sc1\n\t" \
        "Lx0%=:\n\t" \
        "s_memtime s[92:93]\n\t" \
        "s_lshl_b32 s68, s72, 15\n\t" \
        "s_add_u32 s68, s68, s90\n\t" \
        "s_add_u32 s69, s68, 4096\n\t" \
        "buffer_load_dwordx4 v[56:59], %[l16], %[rs], s68 offen offset:0 sc1\n\t" \
        "buffer_load_dwordx4 v[60:63], %[l16], %[rs], s68 offen offset:1024 sc1\n\t" \
        "buffer_load_dwordx4 v[64:67], %[l16], %[rs], s68 offen offset:2048 sc1\n\t" \
        "buffer_load_dwordx4 v[68:71], %[l16], %[rs], s68 offen offset:3072 sc1\n\t" \
        "buffer_load_dwordx4 v[72:75], %[l16], %[rs], s69 offen offset:0 sc1\n\t" \
        "buffer_load_dwordx4 v[76:79], %[l16], %[rs], s69 offen offset:1024 sc1\n\t" \
        "buffer_load_dwordx4 v[80:83], %[l16], %[rs], s69 offen offset:2048 sc1\n\t" \
        "buffer_load_dwordx4 v[84:87], %[l16], %[rs], s69 offen offset:3072 sc1\n\t" \
        "v_readlane_b32 s99, v251, s72\n\t" \
        "s_memtime s[40:41]\n\t" \
        "s_memrealtime s[42:43]\n\t" \
        "s_nop 2\n\t" \
        "s_memrealtime s[44:45]\n\t" \
        "s_nop 2\n\t" \
        "s_memrealtime s[46:47]\n\t" \
        "s_nop 2\n\t" \
        "s_memrealtime s[48:49]\n\t" \
        "s_nop 2\n\t" \
        "s_memrealtime s[50:51]\n\t" \
        "s_nop 2\n\t" \
        "s_memrealtime s[52:53]\n\t" \
        "s_nop 2\n\t" \
        "s_memrealtime s[54:55]\n\t" \
        "s_nop 2\n\t" \
        "s_memrealtime s[56:57]\n\t" \
        "s_waitcnt lgkmcnt(0)\n\t" \
        "s_mov_b32 s41, 0\n\t" \
        "s_cmp_eq_u32 s44, s42\n\t" \
        "s_addc_u32 s41, s41, 0\n\t" \
        "s_cmp_eq_u32 s46, s42\n\t" \
        "s_addc_u32 s41, s41, 0\n\t" \
        "s_cmp_eq_u32 s48, s42\n\t" \
        "s_addc_u32 s41, s41, 0\n\t" \
        "s_cmp_eq_u32 s50, s42\n\t" \
        "s_addc_u32 s41, s41, 0\n\t" \
        "s_cmp_eq_u32 s52, s42\n\t" \
        "s_addc_u32 s41, s41, 0\n\t" \
        "s_cmp_eq_u32 s54, s42\n\t" \
        "s_addc_u32 s41, s41, 0\n\t" \
        "s_cmp_eq_u32 s56, s42\n\t" \
        "s_addc_u32 s41, s41, 0\n\t" \
        "s_lshl_b32 s41, s41, 4\n\t" \
        "s_add_u32 s40, s40, s41\n\t" \
        "s_mul_i32 s41, s42, 24\n\t" \
        "s_sub_u32 s40, s40, s41\n\t" \
        "s_sub_u32 s40, s40, 84\n\t" \
        "s_sub_u32 s41, s40, s63\n\t" \
        "s_max_i32 s41, s41, -352\n\t" \
        "s_min_i32 s41, s41, 0\n\t" \
        "s_mul_i32 s41, s41, 7\n\t" \
        "s_ashr_i32 s41, s41, 5\n\t" \
        "s_add_u32 s97, s40, s41\n\t" \
        "s_mov_b32 s63, s40\n\t" \
        "s_sub_u32 s67, s92, s63\n\t" \
        "s_sub_u32 s64, s67, s62\n\t" \
        "s_add_u32 s65, s98, 48\n\t" \
        "s_min_i32 s64, s64, s65\n\t" \
        "s_sub_u32 s65, s98, s64\n\t" \
        "s_ashr_i32 s65, s65, 2\n\t" \
        "s_sub_u32 s65, s98, s65\n\t" \
        "s_sub_u32 s65, s65, %[dec]\n\t" \
        "s_max_i32 s98, s64, s65\n\t" \
        "s_max_i32 s98, s98, 64\n\t" \
        "s_add_u32 s64, s98, %[gsc]\n\t" \
        "s_min_u32 s73, s66, 1\n\t" \
        "s_lshl_b32 s73, s73, 20\n\t" \
        "s_or_b32 s64, s64, s73\n\t" \
        "s_add_u32 s73, s99, %[so]\n\t" \
        "v_mov_b32 v233, s64\n\t" \
        "s_min_u32 s64, s62, -2\n\t" \
        "v_mov_b32 v232, s64\n\t" \
        "buffer_store_dwordx2 v[232:233], off, %[rs], s73 sc1\n\t" \
        "s_mov_b32 s65, 262144\n\t" \
        "Lr0%=:\n\t" \
        "ds_read_b64 v[236:237], %[asch]\n\t" \
        "s_waitcnt lgkmcnt(0)\n\t" \
        "v_readfirstlane_b32 s62, v236\n\t" \
        "v_readfirstlane_b32 s64, v237\n\t" \
        "s_cmp_eq_u32 s64, s72\n\t" \
        "s_cbranch_scc1 Lrd0%=\n\t" \
        "s_sub_u32 s65, s65, 1\n\t" \
        "s_cbranch_scc0 Lr0%=\n\t" \
        "Lrd0%=:\n\t" \
        "s_mov_b32 s60, s72\n\t" \
        "s_cmp_lt_u32 s60, 63\n\t" \
        "s_cbranch_scc1 Ls0%=\n\t" \
        "s_branch Lend%=\n\t" \
        "Le0%=:\n\t" \
        "v_max_u32 v239, v88, v89\n\t" \
        "v_max3_u32 v239, v239, v90, v91\n\t" \
        "v_max3_u32 v239, v239, v92, v93\n\t" \
        "v_max3_u32 v239, v239, v94, v95\n\t" \
        "v_max3_u32 v239, v239, v96, v97\n\t" \
        "v_max3_u32 v239, v239, v98, v99\n\t" \
        "v_cmp_eq_u32 vcc, -1, v239\n\t" \
        "s_cbranch_vccz Lf0%=\n\t" \
        "s_cmp_gt_u32 s66, 16384\n\t" \
        "s_cbranch_scc1 Lf0%=\n\t" \
        "s_branch Lg0%=\n\t" \
        "Ln0%=:\n\t" \
        "v_max_u32 v239, v88, v89\n\t" \
        "v_max3_u32 v239, v239, v90, v91\n\t" \
        "v_max3_u32 v239, v239, v92, v93\n\t" \
        "v_max3_u32 v239, v239, v94, v95\n\t" \
        "v_max3_u32 v239, v239, v96, v97\n\t" \
        "v_max3_u32 v239, v239, v98, v99\n\t" \
        "v_max3_u32 v239, v239, v100, v101\n\t" \
        "v_max3_u32 v239, v239, v102, v103\n\t" \
        "v_max3_u32 v239, v239, v104, v105\n\t" \
        "v_max3_u32 v239, v239, v106, v107\n\t" \
        "v_max3_u32 v239, v239, v108, v109\n\t" \
        "v_max3_u32 v239, v239, v110, v111\n\t" \
        "v_max3_u32 v239, v239, v112, v113\n\t" \
        "v_max3_u32 v239, v239, v114, v115\n\t" \
        "v_cmp_eq_u32 vcc, -1, v239\n\t" \
        "s_cbranch_vccz Lc0%=\n\t" \
        "s_cmp_gt_u32 s66, 16384\n\t" \
        "s_cbranch_scc1 Lc0%=\n\t" \
        "s_branch Lg0%=\n\t" \
        "Lg0%=:\n\t" \
        "s_add_u32 s66, s66, 1\n\t" \
        "s_waitcnt lgkmcnt(0)\n\t" \
        "ds_read_b128 v[116:119], %[lw] offset:0\n\t" \
        "ds_read_b128 v[120:123], %[lw] offset:1024\n\t" \
        "ds_read_b128 v[124:127], %[lw] offset:2048\n\t" \
        "ds_read_b128 v[128:131], %[lw] offset:3072\n\t" \
        "ds_read_b128 v[132:135], %[lw] offset:4096\n\t" \
        "ds_read_b128 v[136:139], %[lw] offset:5120\n\t" \
        "ds_read_b128 v[140:143], %[lw] offset:6144\n\t" \
        "ds_read_b128 v[144:147], %[lw] offset:7168\n\t" \
        "v_mov_b64 v[148:149], 0\n\t" \
        "v_mov_b64 v[150:151], 0\n\t" \
        "v_mov_b64 v[152:153], 0\n\t" \
        "v_mov_b64 v[154:155], 0\n\t" \
        "v_mov_b64 v[156:157], 0\n\t" \
        "v_mov_b64 v[158:159], 0\n\t" \
        "v_mov_b64 v[160:161], 0\n\t" \
        "v_mov_b64 v[162:163], 0\n\t" \
        "v_mov_b64 v[164:165], 0\n\t" \
        "v_mov_b64 v[166:167], 0\n\t" \
        "v_mov_b64 v[168:169], 0\n\t" \
        "v_mov_b64 v[170:171], 0\n\t" \
        "v_mov_b64 v[172:173], 0\n\t" \
        "v_mov_b64 v[174:175], 0\n\t" \
        "Lp0%=:\n\t" \
        "s_memtime s[92:93]\n\t" \
        "s_waitcnt lgkmcnt(0)\n\t" \
        "s_sub_u32 s73, s92, s63\n\t" \
        "s_movk_i32 s65, 4096\n\t" \
        "La0%=:\n\t" \
        "s_add_u32 s62, s62, 1152\n\t" \
        "s_sub_u32 s65, s65, 1\n\t" \
        "s_cbranch_scc1 La0%=x\n\t" \
        "s_sub_u32 s64, s62, s73\n\t" \
        "s_cmp_lt_i32 s64, -200\n\t" \
        "s_cbranch_scc1 La0%=\n\t" \
        "La0%=x:\n\t" \
        "s_branch Lw0%=\n\t" \
        "Ls1%=:\n\t" \
        "v_readlane_b32 s61, v251, s60\n\t" \
        "s_mov_b32 s66, 0\n\t" \
        "s_add_u32 s72, s60, 1\n\t" \
        "Lw1%=:\n\t" \
        "s_add_u32 s73, s62, s91\n\t" \
        "s_movk_i32 s65, 4096\n\t" \
        "s_add_u32 s94, s73, s97\n\t" \
        "1:\n\t" \
        "s_memtime s[92:93]\n\t" \
        "s_sub_u32 s65, s65, 1\n\t" \
        "s_cbranch_scc1 3f\n\t" \
        "s_waitcnt lgkmcnt(0)\n\t" \
        "s_sub_u32 s64, s94, s92\n\t" \
        "s_cmp_gt_i32 s64, 384\n\t" \
        "s_cbranch_scc1 1b\n\t" \
        "s_cmp_lt_i32 s64, -300\n\t" \
        "s_cbranch_scc1 Lp1%=\n\t" \
        "s_sub_i32 s64, s64, 136\n\t" \
        "s_max_i32 s64, s64, 0\n\t" \
        "s_and_b32 s64, s64, 0xfc\n\t" \
        "s_getpc_b64 s[92:93]\n\t" \
        "2:\n\t" \
        "s_sub_u32 s64, 3f-2b, s64\n\t" \
        "s_add_u32 s92, s92, s64\n\t" \
        "s_addc_u32 s93, s93, 0\n\t" \
        "s_setpc_b64 s[92:93]\n\t" \
        "s_nop 0\n\t" \
        "s_nop 0\n\t" \
        "s_nop 0\n\t" \
        "s_nop 0\n\t" \
        "s_nop 0\n\t" \
        "s_nop 0\n\t" \
        "s_nop 0\n\t" \
        "s_nop 0\n\t" \
        "s_nop 0\n\t" \
        "s_nop 0\n\t" \
        "s_nop 0\n\t" \
        "s_nop 0\n\t" \
        "s_nop 0\n\t" \
        "s_nop 0\n\t" \
        "s_nop 0\n\t" \
        "s_nop 0\n\t" \
        "s_nop 0\n\t" \
        "s_nop 0\n\t" \
        "s_nop 0\n\t" \
        "s_nop 0\n\t" \
        "s_nop 0\n\t" \
        "s_nop 0\n\t" \
        "s_nop 0\n\t" \
        "s_nop 0\n\t" \
        "s_nop 0\n\t" \
        "s_nop 0\n\t" \
        "s_nop 0\n\t" \
        "s_nop 0\n\t" \
        "s_nop 0\n\t" \
        "s_nop 0\n\t" \
        "s_nop 0\n\t" \
        "s_nop 0\n\t" \
        "s_nop 0\n\t" \
        "s_nop 0\n\t" \
        "s_nop 0\n\t" \
        "s_nop 0\n\t" \
        "s_nop 0\n\t" \
        "s_nop 0\n\t" \
        "s_nop 0\n\t" \
        "s_nop 0\n\t" \
        "s_nop 0\n\t" \
        "s_nop 0\n\t" \
        "s_nop 0\n\t" \
        "s_nop 0\n\t" \
        "s_nop 0\n\t" \
        "s_nop 0\n\t" \
        "s_nop 0\n\t" \
        "s_nop 0\n\t" \
        "s_nop 0\n\t" \
        "s_nop 0\n\t" \
        "s_nop 0\n\t" \
        "s_nop 0\n\t" \
        "s_nop 0\n\t" \
        "s_nop 0\n\t" \
        "s_nop 0\n\t" \
        "s_nop 0\n\t" \
        "s_nop 0\n\t" \
        "s_nop 0\n\t" \
        "s_nop 0\n\t" \
        "s_nop 0\n\t" \
        "s_nop 0\n\t" \
        "s_nop 0\n\t" \
        "s_nop 0\n\t" \
        "s_nop 0\n\t" \
        "3:\n\t" \
        "s_waitcnt lgkmcnt(0)\n\t" \
        "Ll1%=:\n\t" \
        "buffer_load_dwordx4 v[88:91], %[m0], %[rs], s61 offen sc1\n\t" \
        "s_nop 13\n\t" \
        "buffer_load_dwordx4 v[92:95], %[m1], %[rs], s61 offen sc1\n\t" \
        "s_nop 13\n\t" \
        "buffer_load_dwordx4 v[96:99], %[m2], %[rs], s61 offen sc1\n\t" \
        "s_nop 13\n\t" \
        "buffer_load_dwordx4 v[100:103], %[m3], %[rs], s61 offen sc1\n\t" \
        "s_nop 13\n\t" \
        "buffer_load_dwordx4 v[104:107], %[m4], %[rs], s61 offen sc1\n\t" \
        "s_nop 13\n\t" \
        "buffer_load_dwordx4 v[108:111], %[m5], %[rs], s61 offen sc1\n\t" \
        "s_nop 13\n\t" \
        "buffer_load_dwordx4 v[112:115], %[m6], %[rs], s61 offen sc1\n\t" \
        "Lm1%=:\n\t" \
        "s_waitcnt vmcnt(6)\n\t" \
        "v_smfmac_f32_16x16x64_bf16 v[160:163], v[88:91], v[116:123], %[idx]\n\t" \
        "ds_read_b128 v[116:119], %[lw] offset:8192\n\t" \
        "v_smfmac_f32_16x16x64_bf16 v[148:151], v[88:91], a[0:7], %[idx]\n\t" \
        "ds_read_b128 v[120:123], %[lw] offset:9216\n\t" \
        "v_smfmac_f32_16x16x64_bf16 v[152:155], v[88:91], a[56:63], %[idx]\n\t" \
        "v_smfmac_f32_16x16x64_bf16 v[156:159], v[88:91], a[112:119], %[idx]\n\t" \
        "s_waitcnt vmcnt(5)\n\t" \
        "v_smfmac_f32_16x16x64_bf16 v[160:163], v[92:95], v[124:131], %[idx]\n\t" \
        "ds_read_b128 v[124:127], %[lw] offset:10240\n\t" \
        "v_smfmac_f32_16x16x64_bf16 v[148:151], v[92:95], a[8:15], %[idx]\n\t" \
        "ds_read_b128 v[128:131], %[lw] offset:11264\n\t" \
        "v_smfmac_f32_16x16x64_bf16 v[152:155], v[92:95], a[64:71], %[idx]\n\t" \
        "v_smfmac_f32_16x16x64_bf16 v[156:159], v[92:95], a[120:127], %[idx]\n\t" \
        "s_waitcnt vmcnt(4)\n\t" \
        "v_smfmac_f32_16x16x64_bf16 v[160:163], v[96:99], v[132:139], %[idx]\n\t" \
        "ds_read_b128 v[132:135], %[lw] offset:12288\n\t" \
        "v_smfmac_f32_16x16x64_bf16 v[148:151], v[96:99], a[16:23], %[idx]\n\t" \
        "ds_read_b128 v[136:139], %[lw] offset:13312\n\t" \
        "v_smfmac_f32_16x16x64_bf16 v[152:155], v[96:99], a[72:79], %[idx]\n\t" \
        "v_smfmac_f32_16x16x64_bf16 v[156:159], v[96:99], a[128:135], %[idx]\n\t" \
        "v_cmp_u_f32 s[70:71], v160, v162\n\t" \
        "s_waitcnt vmcnt(3)\n\t" \
        "v_smfmac_f32_16x16x64_bf16 v[160:163], v[100:103], v[140:147], %[idx]\n\t" \
        "ds_read_b128 v[140:143], %[lw] offset:14336\n\t" \
        "v_smfmac_f32_16x16x64_bf16 v[148:151], v[100:103], a[24:31], %[idx]\n\t" \
        "ds_read_b128 v[144:147], %[lw] offset:15360\n\t" \
        "v_smfmac_f32_16x16x64_bf16 v[152:155], v[100:103], a[80:87], %[idx]\n\t" \
        "s_cmp_lg_u64 s[70:71], 0\n\t" \
        "s_cbranch_scc1 Le1%=\n\t" \
        "Lf1%=:\n\t" \
        "v_smfmac_f32_16x16x64_bf16 v[156:159], v[100:103], a[136:143], %[idx]\n\t" \
        "s_waitcnt vmcnt(2) lgkmcnt(6)\n\t" \
        "v_smfmac_f32_16x16x64_bf16 v[160:163], v[104:107], v[116:123], %[idx]\n\t" \
        "ds_read_b128 v[116:119], %[lw] offset:16384\n\t" \
        "v_smfmac_f32_16x16x64_bf16 v[148:151], v[104:107], a[32:39], %[idx]\n\t" \
        "ds_read_b128 v[120:123], %[lw] offset:17408\n\t" \
        "v_smfmac_f32_16x16x64_bf16 v[152:155], v[104:107], a[88:95], %[idx]\n\t" \
        "v_smfmac_f32_16x16x64_bf16 v[156:159], v[104:107], a[144:151], %[idx]\n\t" \
        "s_waitcnt vmcnt(1) lgkmcnt(6)\n\t" \
        "v_smfmac_f32_16x16x64_bf16 v[160:163], v[108:111], v[124:131], %[idx]\n\t" \
        "ds_read_b128 v[124:127], %[lw] offset:2048\n\t" \
        "v_smfmac_f32_16x16x64_bf16 v[148:151], v[108:111], a[40:47], %[idx]\n\t" \
        "ds_read_b128 v[128:131], %[lw] offset:3072\n\t" \
        "v_smfmac_f32_16x16x64_bf16 v[152:155], v[108:111], a[96:103], %[idx]\n\t" \
        "v_smfmac_f32_16x16x64_bf16 v[156:159], v[108:111], a[152:159], %[idx]\n\t" \
        "s_waitcnt vmcnt(0) lgkmcnt(6)\n\t" \
        "v_smfmac_f32_16x16x64_bf16 v[160:163], v[112:115], v[132:139], %[idx]\n\t" \
        "ds_read_b128 v[132:135], %[lw] offset:4096\n\t" \
        "v_smfmac_f32_16x16x64_bf16 v[148:151], v[112:115], a[48:55], %[idx]\n\t" \
        "ds_read_b128 v[136:139], %[lw] offset:5120\n\t" \
        "v_smfmac_f32_16x16x64_bf16 v[152:155], v[112:115], a[104:111], %[idx]\n\t" \
        "v_smfmac_f32_16x16x64_bf16 v[156:159], v[112:115], a[160:167], %[idx]\n\t" \
        "s_waitcnt lgkmcnt(4)\n\t" \
        "v_add_f32 v232, v160, v162\n\t" \
        "v_smfmac_f32_16x16x64_bf16 v[172:175], v[88:91], v[140:147], %[idx]\n\t" \
        "ds_read_b128 v[140:143], %[lw] offset:6144\n\t" \
        "v_add_f32 v233, v161, v163\n\t" \
        "v_smfmac_f32_16x16x64_bf16 v[164:167], v[88:91], a[168:175], %[idx]\n\t" \
        "ds_read_b128 v[144:147], %[lw] offset:7168\n\t" \
        "v_cmp_u_f32 s[70:71], v232, v233\n\t" \
        "v_smfmac_f32_16x16x64_bf16 v[172:175], v[92:95], v[116:123], %[idx]\n\t" \
        "ds_read_b128 v[116:119], %[lw] offset:0\n\t" \
        "v_cvt_pk_bf16_f32 v237, v232, v233\n\t" \
        "v_smfmac_f32_16x16x64_bf16 v[164:167], v[92:95], a[176:183], %[idx]\n\t" \
        "ds_read_b128 v[120:123], %[lw] offset:1024\n\t" \
        "v_add_f32 v232, v148, v150\n\t" \
        "v_smfmac_f32_16x16x64_bf16 v[172:175], v[96:99], v[192:199], %[idx]\n\t" \
        "s_cmp_lg_u64 s[70:71], 0\n\t" \
        "s_cbranch_scc1 Ln1%=\n\t" \
        "Lc1%=:\n\t" \
        "v_add_f32 v233, v149, v151\n\t" \
        "v_smfmac_f32_16x16x64_bf16 v[164:167], v[96:99], a[184:191], %[idx]\n\t" \
        "ds_write_b32 %[wl1], v237\n\t" \
        "v_cvt_pk_bf16_f32 v234, v232, v233\n\t" \
        "v_smfmac_f32_16x16x64_bf16 v[172:175], v[100:103], v[200:207], %[idx]\n\t" \
        "v_add_f32 v232, v152, v154\n\t" \
        "v_smfmac_f32_16x16x64_bf16 v[164:167], v[100:103], a[192:199], %[idx]\n\t" \
        "v_add_f32 v233, v153, v155\n\t" \
        "v_smfmac_f32_16x16x64_bf16 v[172:175], v[104:107], v[208:215], %[idx]\n\t" \
        "v_cvt_pk_bf16_f32 v235, v232, v233\n\t" \
        "v_smfmac_f32_16x16x64_bf16 v[164:167], v[104:107], a[200:207], %[idx]\n\t" \
        "ds_write_b64 %[awr], v[234:235]\n\t" \
        "v_smfmac_f32_16x16x64_bf16 v[172:175], v[108:111], v[216:223], %[idx]\n\t" \
        "ds_read_b64_tr_b16 v[240:241], %[atr]\n\t" \
        "v_add_f32 v232, v156, v158\n\t" \
        "v_smfmac_f32_16x16x64_bf16 v[164:167], v[108:111], a[208:215], %[idx]\n\t" \
        "v_add_f32 v233, v157, v159\n\t" \
        "v_smfmac_f32_16x16x64_bf16 v[172:175], v[112:115], v[224:231], %[idx]\n\t" \
        "v_cvt_pk_bf16_f32 v236, v232, v233\n\t" \
        "v_smfmac_f32_16x16x64_bf16 v[164:167], v[112:115], a[216:223], %[idx]\n\t" \
        "ds_write_b32 %[wl0], v236\n\t" \
        "v_smfmac_f32_16x16x64_bf16 v[168:171], v[88:91], a[224:231], %[idx]\n\t" \
        "ds_read_b64_tr_b16 v[242:243], %[atr] offset:640\n\t" \
        "v_add_f32 v232, v172, v174\n\t" \
        "v_smfmac_f32_16x16x64_bf16 v[168:171], v[92:95], a[232:239], %[idx]\n\t" \
        "v_add_f32 v233, v173, v175\n\t" \
        "v_smfmac_f32_16x16x64_bf16 v[168:171], v[96:99], a[240:247], %[idx]\n\t" \
        "v_cvt_pk_bf16_f32 v238, v232, v233\n\t" \
        "v_smfmac_f32_16x16x64_bf16 v[168:171], v[100:103], a[248:255], %[idx]\n\t" \
        "ds_write_b32 %[wl2], v238\n\t" \
        "v_smfmac_f32_16x16x64_bf16 v[168:171], v[104:107], v[32:39], %[idx]\n\t" \
        "ds_read_b64_tr_b16 v[246:247], %[atr] offset:1920\n\t" \
        "v_add_f32 v232, v164, v166\n\t" \
        "v_smfmac_f32_16x16x64_bf16 v[168:171], v[108:111], v[40:47], %[idx]\n\t" \
        "v_add_f32 v233, v165, v167\n\t" \
        "v_smfmac_f32_16x16x64_bf16 v[168:171], v[112:115], v[48:55], %[idx]\n\t" \
        "s_waitcnt lgkmcnt(2)\n\t" \
        "v_cvt_pk_bf16_f32 v234, v232, v233\n\t" \
        "v_smfmac_f32_16x16x64_bf16 v[184:187], v[240:243], v[88:95], %[idx]\n\t" \
        "v_mov_b64 v[148:149], 0\n\t" \
        "v_smfmac_f32_16x16x64_bf16 v[176:179], v[240:243], v[56:63], %[idx]\n\t" \
        "s_nop 0\n\t" \
        "v_pk_add_f32 v[232:233], v[168:169], v[170:171]\n\t" \
        "v_cvt_pk_bf16_f32 v235, v232, v233\n\t" \
        "ds_write_b64 %[awr], v[234:235] offset:1280\n\t" \
        "ds_read_b64_tr_b16 v[244:245], %[atr] offset:1280\n\t" \
        "v_smfmac_f32_16x16x64_bf16 v[180:183], v[240:243], v[72:79], %[idx]\n\t" \
        "v_add_f32 v238, v184, v186\n\t" \
        "v_add_f32 v239, v185, v187\n\t" \
        "v_mov_b64 v[150:151], 0\n\t" \
        "v_mov_b64 v[152:153], 0\n\t" \
        "v_mov_b64 v[154:155], 0\n\t" \
        "v_mov_b64 v[156:157], 0\n\t" \
        "v_mov_b64 v[158:159], 0\n\t" \
        "v_mov_b64 v[160:161], 0\n\t" \
        "v_mov_b64 v[162:163], 0\n\t" \
        "v_mov_b64 v[164:165], 0\n\t" \
        "v_mov_b64 v[166:167], 0\n\t" \
        "v_mov_b64 v[168:169], 0\n\t" \
        "v_mov_b64 v[170:171], 0\n\t" \
        "s_waitcnt lgkmcnt(0)\n\t" \
        "v_smfmac_f32_16x16x64_bf16 v[188:191], v[244:247], v[88:95], %[idx]\n\t" \
        "v_smfmac_f32_16x16x64_bf16 v[176:179], v[244:247], v[64:71], %[idx]\n\t" \
        "v_smfmac_f32_16x16x64_bf16 v[180:183], v[244:247], v[80:87], %[idx]\n\t" \
        "s_nop 0\n\t" \
        "v_add_f32 v250, v188, v190\n\t" \
        "v_cndmask_b32_e64 v250, v238, v250, %[hm]\n\t" \
        "v_add_f32 v254, v189, v191\n\t" \
        "v_cndmask_b32_e64 v254, v239, v254, %[hm]\n\t" \
        "v_add_f32_dpp v250, v250, v250 quad_perm:[2,3,0,1] row_mask:0xf bank_mask:0xf\n\t" \
        "v_add_f32 v248, v176, v178\n\t" \
        "v_add_f32_dpp v254, v254, v254 quad_perm:[2,3,0,1] row_mask:0xf bank_mask:0xf\n\t" \
        "v_add_f32 v252, v177, v179\n\t" \
        "v_add_f32 v249, v180, v182\n\t" \
        "ds_write_b96 %[ax], v[248:250] offset:0\n\t" \
        "v_add_f32 v253, v181, v183\n\t" \
        "v_mov_b64 v[172:173], 0\n\t" \
        "v_mov_b64 v[174:175], 0\n\t" \
        "s_waitcnt lgkmcnt(0)\n\t" \
        "s_barrier\n\t" \
        "ds_read_b128 v[96:99], %[ax] offset:3072\n\t" \
        "ds_read_b128 v[100:103], %[ax] offset:4096\n\t" \
        "ds_read_b128 v[104:107], %[ax] offset:5120\n\t" \
        "v_mov_b64 v[176:177], 0\n\t" \
        "v_mov_b64 v[178:179], 0\n\t" \
        "v_mov_b64 v[180:181], 0\n\t" \
        "v_mov_b64 v[182:183], 0\n\t" \
        "v_mov_b64 v[184:185], 0\n\t" \
        "v_mov_b64 v[186:187], 0\n\t" \
        "v_mov_b64 v[188:189], 0\n\t" \
        "v_mov_b64 v[190:191], 0\n\t" \
        "s_waitcnt lgkmcnt(2)\n\t" \
        "v_add_f32 v254, v254, v98\n\t" \
        "v_pk_add_f32 v[252:253], v[252:253], v[96:97]\n\t" \
        "s_waitcnt lgkmcnt(1)\n\t" \
        "v_add_f32 v254, v254, v102\n\t" \
        "v_pk_add_f32 v[252:253], v[252:253], v[100:101]\n\t" \
        "s_waitcnt lgkmcnt(0)\n\t" \
        "v_add_f32 v254, v254, v106\n\t" \
        "v_pk_add_f32 v[252:253], v[252:253], v[104:105]\n\t" \
        "v_max3_f32 v232, v252, v253, v254\n\t" \
        "s_nop 1\n\t" \
        "v_max_f32_dpp v233, v232, v232 quad_perm:[1,0,3,2] row_mask:0xf bank_mask:0xf\n\t" \
        "v_max_f32_dpp v233, v232, v233 quad_perm:[2,3,0,1] row_mask:0xf bank_mask:0xf\n\t" \
        "v_max_f32_dpp v233, v232, v233 quad_perm:[3,2,1,0] row_mask:0xf bank_mask:0xf\n\t" \
        "s_nop 1\n\t" \
        "v_max_f32_dpp v232, v233, v233 row_half_mirror row_mask:0xf bank_mask:0xf\n\t" \
        "v_max_f32_dpp v232, v233, v232 row_mirror row_mask:0xf bank_mask:0xf\n\t" \
        "v_max_f32_dpp v232, v233, v232 row_ror:8 row_mask:0xf bank_mask:0xf\n\t" \
        "v_sub_f32 v254, v254, v232\n\t" \
        "v_pk_add_f32 v[252:253], v[252:253], v[232:233] op_sel_hi:[1,0] neg_lo:[0,1] neg_hi:[0,1]\n\t" \
        "v_exp_f32 v254, v254\n\t" \
        "v_exp_f32 v252, v252\n\t" \
        "v_exp_f32 v253, v253\n\t" \
        "v_fma_f32 v232, v254, 0.5, v252\n\t" \
        "v_add_f32 v232, v232, v253\n\t" \
        "v_mov_b32_dpp v238, v254 quad_perm:[0,0,0,0] row_mask:0xf bank_mask:0xf\n\t" \
        "v_mov_b32_dpp v239, v254 quad_perm:[1,1,1,1] row_mask:0xf bank_mask:0xf\n\t" \
        "v_add_f32_dpp v233, v232, v232 quad_perm:[1,0,3,2] row_mask:0xf bank_mask:0xf\n\t" \
        "v_add_f32_dpp v233, v232, v233 quad_perm:[2,3,0,1] row_mask:0xf bank_mask:0xf\n\t" \
        "v_add_f32_dpp v233, v232, v233 quad_perm:[3,2,1,0] row_mask:0xf bank_mask:0xf\n\t" \
        "s_nop 1\n\t" \
        "v_add_f32_dpp v232, v233, v233 row_half_mirror row_mask:0xf bank_mask:0xf\n\t" \
        "v_add_f32_dpp v232, v233, v232 row_mirror row_mask:0xf bank_mask:0xf\n\t" \
        "v_add_f32_dpp v232, v233, v232 row_ror:8 row_mask:0xf bank_mask:0xf\n\t" \
        "v_rcp_f32 v232, v232\n\t" \
        "s_nop 0\n\t" \
        "v_pk_mul_f32 v[252:253], v[252:253], v[232:233] op_sel_hi:[1,0]\n\t" \
        "v_cvt_pk_bf16_f32 v252, |v252|, |v253|\n\t" \
        "buffer_store_dword v252, %[vpx], %[rs], s61 offen sc1\n\t" \
        "v_pk_mul_f32 v[238:239], v[238:239], v[232:233] op_sel_hi:[1,0]\n\t" \
        "v_cvt_pk_bf16_f32 v238, |v238|, |v239|\n\t" \
        "buffer_store_dword v238, %[vpm], %[rs], s61 offen sc1\n\t" \
        "Lx1%=:\n\t" \
        "s_memtime s[92:93]\n\t" \
        "s_lshl_b32 s68, s72, 15\n\t" \
        "s_add_u32 s68, s68, s90\n\t" \
        "s_add_u32 s69, s68, 4096\n\t" \
        "buffer_load_dwordx4 v[56:59], %[l16], %[rs], s68 offen offset:0 sc1\n\t" \
        "buffer_load_dwordx4 v[60:63], %[l16], %[rs], s68 offen offset:1024 sc1\n\t" \
        "buffer_load_dwordx4 v[64:67], %[l16], %[rs], s68 offen offset:2048 sc1\n\t" \
        "buffer_load_dwordx4 v[68:71], %[l16], %[rs], s68 offen offset:3072 sc1\n\t" \
        "buffer_load_dwordx4 v[72:75], %[l16], %[rs], s69 offen offset:0 sc1\n\t" \
        "buffer_load_dwordx4 v[76:79], %[l16], %[rs], s69 offen offset:1024 sc1\n\t" \
        "buffer_load_dwordx4 v[80:83], %[l16], %[rs], s69 offen offset:2048 sc1\n\t" \
        "buffer_load_dwordx4 v[84:87], %[l16], %[rs], s69 offen offset:3072 sc1\n\t" \
        "v_readlane_b32 s99, v251, s72\n\t" \
        "s_memtime s[40:41]\n\t" \
        "s_memrealtime s[42:43]\n\t" \
        "s_nop 2\n\t" \
        "s_memrealtime s[44:45]\n\t" \
        "s_nop 2\n\t" \
        "s_memrealtime s[46:47]\n\t" \
        "s_nop 2\n\t" \
        "s_memrealtime s[48:49]\n\t" \
        "s_nop 2\n\t" \
        "s_memrealtime s[50:51]\n\t" \
        "s_nop 2\n\t" \
        "s_memrealtime s[52:53]\n\t" \
        "s_nop 2\n\t" \
        "s_memrealtime s[54:55]\n\t" \
        "s_nop 2\n\t" \
        "s_memrealtime s[56:57]\n\t" \
        "s_waitcnt lgkmcnt(0)\n\t" \
        "s_mov_b32 s41, 0\n\t" \
        "s_cmp_eq_u32 s44, s42\n\t" \
        "s_addc_u32 s41, s41, 0\n\t" \
        "s_cmp_eq_u32 s46, s42\n\t" \
        "s_addc_u32 s41, s41, 0\n\t" \
        "s_cmp_eq_u32 s48, s42\n\t" \
        "s_addc_u32 s41, s41, 0\n\t" \
        "s_cmp_eq_u32 s50, s42\n\t" \
        "s_addc_u32 s41, s41, 0\n\t" \
        "s_cmp_eq_u32 s52, s42\n\t" \
        "s_addc_u32 s41, s41, 0\n\t" \
        "s_cmp_eq_u32 s54, s42\n\t" \
        "s_addc_u32 s41, s41, 0\n\t" \
        "s_cmp_eq_u32 s56, s42\n\t" \
        "s_addc_u32 s41, s41, 0\n\t" \
        "s_lshl_b32 s41, s41, 4\n\t" \
        "s_add_u32 s40, s40, s41\n\t" \
        "s_mul_i32 s41, s42, 24\n\t" \
        "s_sub_u32 s40, s40, s41\n\t" \
        "s_sub_u32 s40, s40, 84\n\t" \
        "s_sub_u32 s41, s40, s63\n\t" \
        "s_max_i32 s41, s41, -352\n\t" \
        "s_min_i32 s41, s41, 0\n\t" \
        "s_mul_i32 s41, s41, 7\n\t" \
        "s_ashr_i32 s41, s41, 5\n\t" \
        "s_add_u32 s97, s40, s41\n\t" \
        "s_mov_b32 s63, s40\n\t" \
        "s_sub_u32 s67, s92, s63\n\t" \
        "s_sub_u32 s64, s67, s62\n\t" \
        "s_add_u32 s65, s98, 48\n\t" \
        "s_min_i32 s64, s64, s65\n\t" \
        "s_sub_u32 s65, s98, s64\n\t" \
        "s_ashr_i32 s65, s65, 2\n\t" \
        "s_sub_u32 s65, s98, s65\n\t" \
        "s_sub_u32 s65, s65, %[dec]\n\t" \
        "s_max_i32 s98, s64, s65\n\t" \
        "s_max_i32 s98, s98, 64\n\t" \
        "s_add_u32 s64, s98, %[gsc]\n\t" \
        "s_min_u32 s73, s66, 1\n\t" \
        "s_lshl_b32 s73, s73, 20\n\t" \
        "s_or_b32 s64, s64, s73\n\t" \
        "s_add_u32 s73, s99, %[so]\n\t" \
        "v_mov_b32 v233, s64\n\t" \
        "s_min_u32 s64, s62, -2\n\t" \
        "v_mov_b32 v232, s64\n\t" \
        "buffer_store_dwordx2 v[232:233], off, %[rs], s73 sc1\n\t" \
        "s_mov_b32 s65, 262144\n\t" \
        "Lr1%=:\n\t" \
        "ds_read_b64 v[236:237], %[asch]\n\t" \
        "s_waitcnt lgkmcnt(0)\n\t" \
        "v_readfirstlane_b32 s62, v236\n\t" \
        "v_readfirstlane_b32 s64, v237\n\t" \
        "s_cmp_eq_u32 s64, s72\n\t" \
        "s_cbranch_scc1 Lrd1%=\n\t" \
        "s_sub_u32 s65, s65, 1\n\t" \
        "s_cbranch_scc0 Lr1%=\n\t" \
        "Lrd1%=:\n\t" \
        "s_mov_b32 s60, s72\n\t" \
        "s_cmp_lt_u32 s60, 63\n\t" \
        "s_cbranch_scc1 Ls1%=\n\t" \
        "s_branch Lend%=\n\t" \
        "Le1%=:\n\t" \
        "v_max_u32 v239, v88, v89\n\t" \
        "v_max3_u32 v239, v239, v90, v91\n\t" \
        "v_max3_u32 v239, v239, v92, v93\n\t" \
        "v_max3_u32 v239, v239, v94, v95\n\t" \
        "v_max3_u32 v239, v239, v96, v97\n\t" \
        "v_max3_u32 v239, v239, v98, v99\n\t" \
        "v_cmp_eq_u32 vcc, -1, v239\n\t" \
        "s_cbranch_vccz Lf1%=\n\t" \
        "s_cmp_gt_u32 s66, 16384\n\t" \
        "s_cbranch_scc1 Lf1%=\n\t" \
        "s_branch Lg1%=\n\t" \
        "Ln1%=:\n\t" \
        "v_max_u32 v239, v88, v89\n\t" \
        "v_max3_u32 v239, v239, v90, v91\n\t" \
        "v_max3_u32 v239, v239, v92, v93\n\t" \
        "v_max3_u32 v239, v239, v94, v95\n\t" \
        "v_max3_u32 v239, v239, v96, v97\n\t" \
        "v_max3_u32 v239, v239, v98, v99\n\t" \
        "v_max3_u32 v239, v239, v100, v101\n\t" \
        "v_max3_u32 v239, v239, v102, v103\n\t" \
        "v_max3_u32 v239, v239, v104, v105\n\t" \
        "v_max3_u32 v239, v239, v106, v107\n\t" \
        "v_max3_u32 v239, v239, v108, v109\n\t" \
        "v_max3_u32 v239, v239, v110, v111\n\t" \
        "v_max3_u32 v239, v239, v112, v113\n\t" \
        "v_max3_u32 v239, v239, v114, v115\n\t" \
        "v_cmp_eq_u32 vcc, -1, v239\n\t" \
        "s_cbranch_vccz Lc1%=\n\t" \
        "s_cmp_gt_u32 s66, 16384\n\t" \
        "s_cbranch_scc1 Lc1%=\n\t" \
        "s_branch Lg1%=\n\t" \
        "Lg1%=:\n\t" \
        "s_add_u32 s66, s66, 1\n\t" \
        "s_waitcnt lgkmcnt(0)\n\t" \
        "ds_read_b128 v[116:119], %[lw] offset:0\n\t" \
        "ds_read_b128 v[120:123], %[lw] offset:1024\n\t" \
        "ds_read_b128 v[124:127], %[lw] offset:2048\n\t" \
        "ds_read_b128 v[128:131], %[lw] offset:3072\n\t" \
        "ds_read_b128 v[132:135], %[lw] offset:4096\n\t" \
        "ds_read_b128 v[136:139], %[lw] offset:5120\n\t" \
        "ds_read_b128 v[140:143], %[lw] offset:6144\n\t" \
        "ds_read_b128 v[144:147], %[lw] offset:7168\n\t" \
        "v_mov_b64 v[148:149], 0\n\t" \
        "v_mov_b64 v[150:151], 0\n\t" \
        "v_mov_b64 v[152:153], 0\n\t" \
        "v_mov_b64 v[154:155], 0\n\t" \
        "v_mov_b64 v[156:157], 0\n\t" \
        "v_mov_b64 v[158:159], 0\n\t" \
        "v_mov_b64 v[160:161], 0\n\t" \
        "v_mov_b64 v[162:163], 0\n\t" \
        "v_mov_b64 v[164:165], 0\n\t" \
        "v_mov_b64 v[166:167], 0\n\t" \
        "v_mov_b64 v[168:169], 0\n\t" \
        "v_mov_b64 v[170:171], 0\n\t" \
        "v_mov_b64 v[172:173], 0\n\t" \
        "v_mov_b64 v[174:175], 0\n\t" \
        "Lp1%=:\n\t" \
        "s_memtime s[92:93]\n\t" \
        "s_waitcnt lgkmcnt(0)\n\t" \
        "s_sub_u32 s73, s92, s63\n\t" \
        "s_movk_i32 s65, 4096\n\t" \
        "La1%=:\n\t" \
        "s_add_u32 s62, s62, 1152\n\t" \
        "s_sub_u32 s65, s65, 1\n\t" \
        "s_cbranch_scc1 La1%=x\n\t" \
        "s_sub_u32 s64, s62, s73\n\t" \
        "s_cmp_lt_i32 s64, -200\n\t" \
        "s_cbranch_scc1 La1%=\n\t" \
        "La1%=x:\n\t" \
        "s_branch Lw1%=\n\t" \
        "Ls2%=:\n\t" \
        "v_readlane_b32 s61, v251, s60\n\t" \
        "s_mov_b32 s66, 0\n\t" \
        "s_add_u32 s72, s60, 1\n\t" \
        "Lw2%=:\n\t" \
        "s_add_u32 s73, s62, s91\n\t" \
        "s_movk_i32 s65, 4096\n\t" \
        "s_add_u32 s94, s73, s97\n\t" \
        "1:\n\t" \
        "s_memtime s[92:93]\n\t" \
        "s_sub_u32 s65, s65, 1\n\t" \
        "s_cbranch_scc1 3f\n\t" \
        "s_waitcnt lgkmcnt(0)\n\t" \
        "s_sub_u32 s64, s94, s92\n\t" \
        "s_cmp_gt_i32 s64, 384\n\t" \
        "s_cbranch_scc1 1b\n\t" \
        "s_cmp_lt_i32 s64, -300\n\t" \
        "s_cbranch_scc1 Lp2%=\n\t" \
        "s_sub_i32 s64, s64, 136\n\t" \
        "s_max_i32 s64, s64, 0\n\t" \
        "s_and_b32 s64, s64, 0xfc\n\t" \
        "s_getpc_b64 s[92:93]\n\t" \
        "2:\n\t" \
        "s_sub_u32 s64, 3f-2b, s64\n\t" \
        "s_add_u32 s92, s92, s64\n\t" \
        "s_addc_u32 s93, s93, 0\n\t" \
        "s_setpc_b64 s[92:93]\n\t" \
        "s_nop 0\n\t" \
        "s_nop 0\n\t" \
        "s_nop 0\n\t" \
        "s_nop 0\n\t" \
        "s_nop 0\n\t" \
        "s_nop 0\n\t" \
        "s_nop 0\n\t" \
        "s_nop 0\n\t" \
        "s_nop 0\n\t" \
        "s_nop 0\n\t" \
        "s_nop 0\n\t" \
        "s_nop 0\n\t" \
        "s_nop 0\n\t" \
        "s_nop 0\n\t" \
        "s_nop 0\n\t" \
        "s_nop 0\n\t" \
        "s_nop 0\n\t" \
        "s_nop 0\n\t" \
        "s_nop 0\n\t" \
        "s_nop 0\n\t" \
        "s_nop 0\n\t" \
        "s_nop 0\n\t" \
        "s_nop 0\n\t" \
        "s_nop 0\n\t" \
        "s_nop 0\n\t" \
        "s_nop 0\n\t" \
        "s_nop 0\n\t" \
        "s_nop 0\n\t" \
        "s_nop 0\n\t" \
        "s_nop 0\n\t" \
        "s_nop 0\n\t" \
        "s_nop 0\n\t" \
        "s_nop 0\n\t" \
        "s_nop 0\n\t" \
        "s_nop 0\n\t" \
        "s_nop 0\n\t" \
        "s_nop 0\n\t" \
        "s_nop 0\n\t" \
        "s_nop 0\n\t" \
        "s_nop 0\n\t" \
        "s_nop 0\n\t" \
        "s_nop 0\n\t" \
        "s_nop 0\n\t" \
        "s_nop 0\n\t" \
        "s_nop 0\n\t" \
        "s_nop 0\n\t" \
        "s_nop 0\n\t" \
        "s_nop 0\n\t" \
        "s_nop 0\n\t" \
        "s_nop 0\n\t" \
        "s_nop 0\n\t" \
        "s_nop 0\n\t" \
        "s_nop 0\n\t" \
        "s_nop 0\n\t" \
        "s_nop 0\n\t" \
        "s_nop 0\n\t" \
        "s_nop 0\n\t" \
        "s_nop 0\n\t" \
        "s_nop 0\n\t" \
        "s_nop 0\n\t" \
        "s_nop 0\n\t" \
        "s_nop 0\n\t" \
        "s_nop 0\n\t" \
        "s_nop 0\n\t" \
        "3:\n\t" \
        "s_waitcnt lgkmcnt(0)\n\t" \
        "Ll2%=:\n\t" \
        "buffer_load_dwordx4 v[88:91], %[m0], %[rs], s61 offen sc1\n\t" \
        "s_nop 13\n\t" \
        "buffer_load_dwordx4 v[92:95], %[m1], %[rs], s61 offen sc1\n\t" \
        "s_nop 13\n\t" \
        "buffer_load_dwordx4 v[96:99], %[m2], %[rs], s61 offen sc1\n\t" \
        "s_nop 13\n\t" \
        "buffer_load_dwordx4 v[100:103], %[m3], %[rs], s61 offen sc1\n\t" \
        "s_nop 13\n\t" \
        "buffer_load_dwordx4 v[104:107], %[m4], %[rs], s61 offen sc1\n\t" \
        "s_nop 13\n\t" \
        "buffer_load_dwordx4 v[108:111], %[m5], %[rs], s61 offen sc1\n\t" \
        "s_nop 13\n\t" \
        "buffer_load_dwordx4 v[112:115], %[m6], %[rs], s61 offen sc1\n\t" \
        "Lm2%=:\n\t" \
        "s_waitcnt vmcnt(6)\n\t" \
        "v_smfmac_f32_16x16x64_bf16 v[160:163], v[88:91], v[116:123], %[idx]\n\t" \
        "ds_read_b128 v[116:119], %[lw] offset:8192\n\t" \
        "v_smfmac_f32_16x16x64_bf16 v[148:151], v[88:91], a[0:7], %[idx]\n\t" \
        "ds_read_b128 v[120:123], %[lw] offset:9216\n\t" \
        "v_smfmac_f32_16x16x64_bf16 v[152:155], v[88:91], a[56:63], %[idx]\n\t" \
        "v_smfmac_f32_16x16x64_bf16 v[156:159], v[88:91], a[112:119], %[idx]\n\t" \
        "s_waitcnt vmcnt(5)\n\t" \
        "v_smfmac_f32_16x16x64_bf16 v[160:163], v[92:95], v[124:131], %[idx]\n\t" \
        "ds_read_b128 v[124:127], %[lw] offset:10240\n\t" \
        "v_smfmac_f32_16x16x64_bf16 v[148:151], v[92:95], a[8:15], %[idx]\n\t" \
        "ds_read_b128 v[128:131], %[lw] offset:11264\n\t" \
        "v_smfmac_f32_16x16x64_bf16 v[152:155], v[92:95], a[64:71], %[idx]\n\t" \
        "v_smfmac_f32_16x16x64_bf16 v[156:159], v[92:95], a[120:127], %[idx]\n\t" \
        "s_waitcnt vmcnt(4)\n\t" \
        "v_smfmac_f32_16x16x64_bf16 v[160:163], v[96:99], v[132:139], %[idx]\n\t" \
        "ds_read_b128 v[132:135], %[lw] offset:12288\n\t" \
        "v_smfmac_f32_16x16x64_bf16 v[148:151], v[96:99], a[16:23], %[idx]\n\t" \
        "ds_read_b128 v[136:139], %[lw] offset:13312\n\t" \
        "v_smfmac_f32_16x16x64_bf16 v[152:155], v[96:99], a[72:79], %[idx]\n\t" \
        "v_smfmac_f32_16x16x64_bf16 v[156:159], v[96:99], a[128:135], %[idx]\n\t" \
        "v_cmp_u_f32 s[70:71], v160, v162\n\t" \
        "s_waitcnt vmcnt(3)\n\t" \
        "v_smfmac_f32_16x16x64_bf16 v[160:163], v[100:103], v[140:147], %[idx]\n\t" \
        "ds_read_b128 v[140:143], %[lw] offset:14336\n\t" \
        "v_smfmac_f32_16x16x64_bf16 v[148:151], v[100:103], a[24:31], %[idx]\n\t" \
        "ds_read_b128 v[144:147], %[lw] offset:15360\n\t" \
        "v_smfmac_f32_16x16x64_bf16 v[152:155], v[100:103], a[80:87], %[idx]\n\t" \
        "s_cmp_lg_u64 s[70:71], 0\n\t" \
        "s_cbranch_scc1 Le2%=\n\t" \
        "Lf2%=:\n\t" \
        "v_smfmac_f32_16x16x64_bf16 v[156:159], v[100:103], a[136:143], %[idx]\n\t" \
        "s_waitcnt vmcnt(2) lgkmcnt(6)\n\t" \
        "v_smfmac_f32_16x16x64_bf16 v[160:163], v[104:107], v[116:123], %[idx]\n\t" \
        "ds_read_b128 v[116:119], %[lw] offset:16384\n\t" \
        "v_smfmac_f32_16x16x64_bf16 v[148:151], v[104:107], a[32:39], %[idx]\n\t" \
        "ds_read_b128 v[120:123], %[lw] offset:17408\n\t" \
        "v_smfmac_f32_16x16x64_bf16 v[152:155], v[104:107], a[88:95], %[idx]\n\t" \
        "v_smfmac_f32_16x16x64_bf16 v[156:159], v[104:107], a[144:151], %[idx]\n\t" \
        "s_waitcnt vmcnt(1) lgkmcnt(6)\n\t" \
        "v_smfmac_f32_16x16x64_bf16 v[160:163], v[108:111], v[124:131], %[idx]\n\t" \
        "ds_read_b128 v[124:127], %[lw] offset:2048\n\t" \
        "v_smfmac_f32_16x16x64_bf16 v[148:151], v[108:111], a[40:47], %[idx]\n\t" \
        "ds_read_b128 v[128:131], %[lw] offset:3072\n\t" \
        "v_smfmac_f32_16x16x64_bf16 v[152:155], v[108:111], a[96:103], %[idx]\n\t" \
        "v_smfmac_f32_16x16x64_bf16 v[156:159], v[108:111], a[152:159], %[idx]\n\t" \
        "s_waitcnt vmcnt(0) lgkmcnt(6)\n\t" \
        "v_smfmac_f32_16x16x64_bf16 v[160:163], v[112:115], v[132:139], %[idx]\n\t" \
        "ds_read_b128 v[132:135], %[lw] offset:4096\n\t" \
        "v_smfmac_f32_16x16x64_bf16 v[148:151], v[112:115], a[48:55], %[idx]\n\t" \
        "ds_read_b128 v[136:139], %[lw] offset:5120\n\t" \
        "v_smfmac_f32_16x16x64_bf16 v[152:155], v[112:115], a[104:111], %[idx]\n\t" \
        "v_smfmac_f32_16x16x64_bf16 v[156:159], v[112:115], a[160:167], %[idx]\n\t" \
        "s_waitcnt lgkmcnt(4)\n\t" \
        "v_add_f32 v232, v160, v162\n\t" \
        "v_smfmac_f32_16x16x64_bf16 v[172:175], v[88:91], v[140:147], %[idx]\n\t" \
        "ds_read_b128 v[140:143], %[lw] offset:6144\n\t" \
        "v_add_f32 v233, v161, v163\n\t" \
        "v_smfmac_f32_16x16x64_bf16 v[164:167], v[88:91], a[168:175], %[idx]\n\t" \
        "ds_read_b128 v[144:147], %[lw] offset:7168\n\t" \
        "v_cmp_u_f32 s[70:71], v232, v233\n\t" \
        "v_smfmac_f32_16x16x64_bf16 v[172:175], v[92:95], v[116:123], %[idx]\n\t" \
        "ds_read_b128 v[116:119], %[lw] offset:0\n\t" \
        "v_cvt_pk_bf16_f32 v237, v232, v233\n\t" \
        "v_smfmac_f32_16x16x64_bf16 v[164:167], v[92:95], a[176:183], %[idx]\n\t" \
        "ds_read_b128 v[120:123], %[lw] offset:1024\n\t" \
        "v_add_f32 v232, v148, v150\n\t" \
        "v_smfmac_f32_16x16x64_bf16 v[172:175], v[96:99], v[192:199], %[idx]\n\t" \
        "s_cmp_lg_u64 s[70:71], 0\n\t" \
        "s_cbranch_scc1 Ln2%=\n\t" \
        "Lc2%=:\n\t" \
        "v_add_f32 v233, v149, v151\n\t" \
        "v_smfmac_f32_16x16x64_bf16 v[164:167], v[96:99], a[184:191], %[idx]\n\t" \
        "ds_write_b32 %[wl1], v237\n\t" \
        "v_cvt_pk_bf16_f32 v234, v232, v233\n\t" \
        "v_smfmac_f32_16x16x64_bf16 v[172:175], v[100:103], v[200:207], %[idx]\n\t" \
        "v_add_f32 v232, v152, v154\n\t" \
        "v_smfmac_f32_16x16x64_bf16 v[164:167], v[100:103], a[192:199], %[idx]\n\t" \
        "v_add_f32 v233, v153, v155\n\t" \
        "v_smfmac_f32_16x16x64_bf16 v[172:175], v[104:107], v[208:215], %[idx]\n\t" \
        "v_cvt_pk_bf16_f32 v235, v232, v233\n\t" \
        "v_smfmac_f32_16x16x64_bf16 v[164:167], v[104:107], a[200:207], %[idx]\n\t" \
        "ds_write_b64 %[awr], v[234:235]\n\t" \
        "v_smfmac_f32_16x16x64_bf16 v[172:175], v[108:111], v[216:223], %[idx]\n\t" \
        "ds_read_b64_tr_b16 v[240:241], %[atr]\n\t" \
        "v_add_f32 v232, v156, v158\n\t" \
        "v_smfmac_f32_16x16x64_bf16 v[164:167], v[108:111], a[208:215], %[idx]\n\t" \
        "v_add_f32 v233, v157, v159\n\t" \
        "v_smfmac_f32_16x16x64_bf16 v[172:175], v[112:115], v[224:231], %[idx]\n\t" \
        "v_cvt_pk_bf16_f32 v236, v232, v233\n\t" \
        "v_smfmac_f32_16x16x64_bf16 v[164:167], v[112:115], a[216:223], %[idx]\n\t" \
        "ds_write_b32 %[wl0], v236\n\t" \
        "v_smfmac_f32_16x16x64_bf16 v[168:171], v[88:91], a[224:231], %[idx]\n\t" \
        "ds_read_b64_tr_b16 v[242:243], %[atr] offset:640\n\t" \
        "v_add_f32 v232, v172, v174\n\t" \
        "v_smfmac_f32_16x16x64_bf16 v[168:171], v[92:95], a[232:239], %[idx]\n\t" \
        "buffer_load_dwordx2 v[236:237], %[vpr], %[rs], s61 offen sc1\n\t" \
        "v_add_f32 v233, v173, v175\n\t" \
        "v_smfmac_f32_16x16x64_bf16 v[168:171], v[96:99], a[240:247], %[idx]\n\t" \
        "v_cvt_pk_bf16_f32 v238, v232, v233\n\t" \
        "v_smfmac_f32_16x16x64_bf16 v[168:171], v[100:103], a[248:255], %[idx]\n\t" \
        "ds_write_b32 %[wl2], v238\n\t" \
        "v_smfmac_f32_16x16x64_bf16 v[168:171], v[104:107], v[32:39], %[idx]\n\t" \
        "ds_read_b64_tr_b16 v[246:247], %[atr] offset:1920\n\t" \
        "v_add_f32 v232, v164, v166\n\t" \
        "v_smfmac_f32_16x16x64_bf16 v[168:171], v[108:111], v[40:47], %[idx]\n\t" \
        "v_add_f32 v233, v165, v167\n\t" \
        "v_smfmac_f32_16x16x64_bf16 v[168:171], v[112:115], v[48:55], %[idx]\n\t" \
        "s_waitcnt lgkmcnt(2)\n\t" \
        "v_cvt_pk_bf16_f32 v234, v232, v233\n\t" \
        "v_smfmac_f32_16x16x64_bf16 v[184:187], v[240:243], v[88:95], %[idx]\n\t" \
        "v_mov_b64 v[148:149], 0\n\t" \
        "v_smfmac_f32_16x16x64_bf16 v[176:179], v[240:243], v[56:63], %[idx]\n\t" \
        "s_nop 0\n\t" \
        "v_pk_add_f32 v[232:233], v[168:169], v[170:171]\n\t" \
        "v_cvt_pk_bf16_f32 v235, v232, v233\n\t" \
        "ds_write_b64 %[awr], v[234:235] offset:1280\n\t" \
        "ds_read_b64_tr_b16 v[244:245], %[atr] offset:1280\n\t" \
        "v_smfmac_f32_16x16x64_bf16 v[180:183], v[240:243], v[72:79], %[idx]\n\t" \
        "v_add_f32 v238, v184, v186\n\t" \
        "v_add_f32 v239, v185, v187\n\t" \
        "v_mov_b64 v[150:151], 0\n\t" \
        "v_mov_b64 v[152:153], 0\n\t" \
        "v_mov_b64 v[154:155], 0\n\t" \
        "v_mov_b64 v[156:157], 0\n\t" \
        "v_mov_b64 v[158:159], 0\n\t" \
        "v_mov_b64 v[160:161], 0\n\t" \
        "v_mov_b64 v[162:163], 0\n\t" \
        "v_mov_b64 v[164:165], 0\n\t" \
        "v_mov_b64 v[166:167], 0\n\t" \
        "v_mov_b64 v[168:169], 0\n\t" \
        "v_mov_b64 v[170:171], 0\n\t" \
        "s_waitcnt lgkmcnt(0)\n\t" \
        "v_smfmac_f32_16x16x64_bf16 v[188:191], v[244:247], v[88:95], %[idx]\n\t" \
        "v_smfmac_f32_16x16x64_bf16 v[176:179], v[244:247], v[64:71], %[idx]\n\t" \
        "v_smfmac_f32_16x16x64_bf16 v[180:183], v[244:247], v[80:87], %[idx]\n\t" \
        "s_nop 0\n\t" \
        "v_add_f32 v250, v188, v190\n\t" \
        "v_cndmask_b32_e64 v250, v238, v250, %[hm]\n\t" \
        "v_add_f32 v254, v189, v191\n\t" \
        "v_cndmask_b32_e64 v254, v239, v254, %[hm]\n\t" \
        "v_add_f32_dpp v250, v250, v250 quad_perm:[2,3,0,1] row_mask:0xf bank_mask:0xf\n\t" \
        "v_add_f32 v248, v176, v178\n\t" \
        "v_add_f32_dpp v254, v254, v254 quad_perm:[2,3,0,1] row_mask:0xf bank_mask:0xf\n\t" \
        "v_add_f32 v252, v177, v179\n\t" \
        "v_add_f32 v249, v180, v182\n\t" \
        "ds_write_b96 %[ax], v[248:250] offset:1024\n\t" \
        "v_add_f32 v253, v181, v183\n\t" \
        "ds_write_b96 %[ax], v[252:254] offset:4096\n\t" \
        "s_waitcnt lgkmcnt(0)\n\t" \
        "s_barrier\n\t" \
        "Lx2%=:\n\t" \
        "s_memtime s[92:93]\n\t" \
        "s_lshl_b32 s68, s72, 15\n\t" \
        "s_add_u32 s68, s68, s90\n\t" \
        "s_add_u32 s69, s68, 4096\n\t" \
        "buffer_load_dwordx4 v[56:59], %[l16], %[rs], s68 offen offset:0 sc1\n\t" \
        "buffer_load_dwordx4 v[60:63], %[l16], %[rs], s68 offen offset:1024 sc1\n\t" \
        "buffer_load_dwordx4 v[64:67], %[l16], %[rs], s68 offen offset:2048 sc1\n\t" \
        "buffer_load_dwordx4 v[68:71], %[l16], %[rs], s68 offen offset:3072 sc1\n\t" \
        "buffer_load_dwordx4 v[72:75], %[l16], %[rs], s69 offen offset:0 sc1\n\t" \
        "buffer_load_dwordx4 v[76:79], %[l16], %[rs], s69 offen offset:1024 sc1\n\t" \
        "buffer_load_dwordx4 v[80:83], %[l16], %[rs], s69 offen offset:2048 sc1\n\t" \
        "buffer_load_dwordx4 v[84:87], %[l16], %[rs], s69 offen offset:3072 sc1\n\t" \
        "s_waitcnt vmcnt(8)\n\t" \
        "s_movk_i32 s65, 16384\n\t" \
        "Lr2%=:\n\t" \
        "v_max_u32 v232, v236, v237\n\t" \
        "v_cmp_eq_u32 vcc, -1, v232\n\t" \
        "s_cbranch_vccz Lrd2%=\n\t" \
        "buffer_load_dwordx2 v[236:237], %[vpr], %[rs], s61 offen sc1\n\t" \
        "s_waitcnt vmcnt(0)\n\t" \
        "s_sub_u32 s65, s65, 1\n\t" \
        "s_cbranch_scc0 Lr2%=\n\t" \
        "Lrd2%=:\n\t" \
        "v_subrev_u32 v236, s62, v236\n\t" \
        "s_nop 0\n\t" \
        "v_max_i32_dpp v237, v237, v237 quad_perm:[1,0,3,2] row_mask:0xf bank_mask:0xf\n\t" \
        "v_max_i32_dpp v236, v236, v236 quad_perm:[1,0,3,2] row_mask:0xf bank_mask:0xf\n\t" \
        "s_nop 0\n\t" \
        "v_max_i32_dpp v237, v237, v237 quad_perm:[2,3,0,1] row_mask:0xf bank_mask:0xf\n\t" \
        "v_max_i32_dpp v236, v236, v236 quad_perm:[2,3,0,1] row_mask:0xf bank_mask:0xf\n\t" \
        "s_nop 0\n\t" \
        "v_max_i32_dpp v237, v237, v237 row_half_mirror row_mask:0xf bank_mask:0xf\n\t" \
        "v_max_i32_dpp v236, v236, v236 row_half_mirror row_mask:0xf bank_mask:0xf\n\t" \
        "s_nop 0\n\t" \
        "v_max_i32_dpp v237, v237, v237 row_mirror row_mask:0xf bank_mask:0xf\n\t" \
        "v_max_i32_dpp v236, v236, v236 row_mirror row_mask:0xf bank_mask:0xf\n\t" \
        "s_nop 0\n\t" \
        "v_readlane_b32 s64, v236, 0\n\t" \
        "v_readlane_b32 s73, v236, 16\n\t" \
        "s_max_i32 s64, s64, s73\n\t" \
        "v_readlane_b32 s65, v236, 32\n\t" \
        "v_readlane_b32 s73, v236, 48\n\t" \
        "s_max_i32 s64, s64, s73\n\t" \
        "s_max_i32 s64, s64, s65\n\t" \
        "s_add_u32 s62, s62, s64\n\t" \
        "v_readlane_b32 s64, v237, 0\n\t" \
        "v_readlane_b32 s73, v237, 16\n\t" \
        "s_max_i32 s64, s64, s73\n\t" \
        "s_add_u32 s62, s62, s64\n\t" \
        "v_readlane_b32 s64, v237, 32\n\t" \
        "v_readlane_b32 s73, v237, 48\n\t" \
        "s_max_i32 s64, s64, s73\n\t" \
        "s_and_b32 s64, s64, 0xfffff\n\t" \
        "s_add_u32 s62, s62, s64\n\t" \
        "Lq2%=:\n\t" \
        "v_mov_b32 v236, s62\n\t" \
        "v_mov_b32 v237, s72\n\t" \
        "ds_write_b64 %[asch], v[236:237]\n\t" \
        "v_mov_b64 v[172:173], 0\n\t" \
        "v_mov_b64 v[174:175], 0\n\t" \
        "v_mov_b64 v[176:177], 0\n\t" \
        "v_mov_b64 v[178:179], 0\n\t" \
        "v_mov_b64 v[180:181], 0\n\t" \
        "v_mov_b64 v[182:183], 0\n\t" \
        "v_mov_b64 v[184:185], 0\n\t" \
        "v_mov_b64 v[186:187], 0\n\t" \
        "v_mov_b64 v[188:189], 0\n\t" \
        "v_mov_b64 v[190:191], 0\n\t" \
        "s_sleep 3\n\t" \
        "s_memtime s[40:41]\n\t" \
        "s_memrealtime s[42:43]\n\t" \
        "s_nop 2\n\t" \
        "s_memrealtime s[44:45]\n\t" \
        "s_nop 2\n\t" \
        "s_memrealtime s[46:47]\n\t" \
        "s_nop 2\n\t" \
        "s_memrealtime s[48:49]\n\t" \
        "s_nop 2\n\t" \
        "s_memrealtime s[50:51]\n\t" \
        "s_nop 2\n\t" \
        "s_memrealtime s[52:53]\n\t" \
        "s_nop 2\n\t" \
        "s_memrealtime s[54:55]\n\t" \
        "s_nop 2\n\t" \
        "s_memrealtime s[56:57]\n\t" \
        "s_waitcnt lgkmcnt(0)\n\t" \
        "s_mov_b32 s41, 0\n\t" \
        "s_cmp_eq_u32 s44, s42\n\t" \
        "s_addc_u32 s41, s41, 0\n\t" \
        "s_cmp_eq_u32 s46, s42\n\t" \
        "s_addc_u32 s41, s41, 0\n\t" \
        "s_cmp_eq_u32 s48, s42\n\t" \
        "s_addc_u32 s41, s41, 0\n\t" \
        "s_cmp_eq_u32 s50, s42\n\t" \
        "s_addc_u32 s41, s41, 0\n\t" \
        "s_cmp_eq_u32 s52, s42\n\t" \
        "s_addc_u32 s41, s41, 0\n\t" \
        "s_cmp_eq_u32 s54, s42\n\t" \
        "s_addc_u32 s41, s41, 0\n\t" \
        "s_cmp_eq_u32 s56, s42\n\t" \
        "s_addc_u32 s41, s41, 0\n\t" \
        "s_lshl_b32 s41, s41, 4\n\t" \
        "s_add_u32 s40, s40, s41\n\t" \
        "s_mul_i32 s41, s42, 24\n\t" \
        "s_sub_u32 s40, s40, s41\n\t" \
        "s_sub_u32 s40, s40, 84\n\t" \
        "s_sub_u32 s41, s40, s63\n\t" \
        "s_max_i32 s41, s41, -352\n\t" \
        "s_min_i32 s41, s41, 0\n\t" \
        "s_mul_i32 s41, s41, 5\n\t" \
        "s_ashr_i32 s41, s41, 5\n\t" \
        "s_add_u32 s97, s40, s41\n\t" \
        "s_mov_b32 s63, s40\n\t" \
        "s_mov_b32 s60, s72\n\t" \
        "s_cmp_lt_u32 s60, 63\n\t" \
        "s_cbranch_scc1 Ls2%=\n\t" \
        "s_branch Lend%=\n\t" \
        "Le2%=:\n\t" \
        "v_max_u32 v239, v88, v89\n\t" \
        "v_max3_u32 v239, v239, v90, v91\n\t" \
        "v_max3_u32 v239, v239, v92, v93\n\t" \
        "v_max3_u32 v239, v239, v94, v95\n\t" \
        "v_max3_u32 v239, v239, v96, v97\n\t" \
        "v_max3_u32 v239, v239, v98, v99\n\t" \
        "v_cmp_eq_u32 vcc, -1, v239\n\t" \
        "s_cbranch_vccz Lf2%=\n\t" \
        "s_cmp_gt_u32 s66, 16384\n\t" \
        "s_cbranch_scc1 Lf2%=\n\t" \
        "s_branch Lg2%=\n\t" \
        "Ln2%=:\n\t" \
        "v_max_u32 v239, v88, v89\n\t" \
        "v_max3_u32 v239, v239, v90, v91\n\t" \
        "v_max3_u32 v239, v239, v92, v93\n\t" \
        "v_max3_u32 v239, v239, v94, v95\n\t" \
        "v_max3_u32 v239, v239, v96, v97\n\t" \
        "v_max3_u32 v239, v239, v98, v99\n\t" \
        "v_max3_u32 v239, v239, v100, v101\n\t" \
        "v_max3_u32 v239, v239, v102, v103\n\t" \
        "v_max3_u32 v239, v239, v104, v105\n\t" \
        "v_max3_u32 v239, v239, v106, v107\n\t" \
        "v_max3_u32 v239, v239, v108, v109\n\t" \
        "v_max3_u32 v239, v239, v110, v111\n\t" \
        "v_max3_u32 v239, v239, v112, v113\n\t" \
        "v_max3_u32 v239, v239, v114, v115\n\t" \
        "v_cmp_eq_u32 vcc, -1, v239\n\t" \
        "s_cbranch_vccz Lc2%=\n\t" \
        "s_cmp_gt_u32 s66, 16384\n\t" \
        "s_cbranch_scc1 Lc2%=\n\t" \
        "s_branch Lg2%=\n\t" \
        "Lg2%=:\n\t" \
        "s_add_u32 s66, s66, 1\n\t" \
        "s_waitcnt lgkmcnt(0)\n\t" \
        "ds_read_b128 v[116:119], %[lw] offset:0\n\t" \
        "ds_read_b128 v[120:123], %[lw] offset:1024\n\t" \
        "ds_read_b128 v[124:127], %[lw] offset:2048\n\t" \
        "ds_read_b128 v[128:131], %[lw] offset:3072\n\t" \
        "ds_read_b128 v[132:135], %[lw] offset:4096\n\t" \
        "ds_read_b128 v[136:139], %[lw] offset:5120\n\t" \
        "ds_read_b128 v[140:143], %[lw] offset:6144\n\t" \
        "ds_read_b128 v[144:147], %[lw] offset:7168\n\t" \
        "v_mov_b64 v[148:149], 0\n\t" \
        "v_mov_b64 v[150:151], 0\n\t" \
        "v_mov_b64 v[152:153], 0\n\t" \
        "v_mov_b64 v[154:155], 0\n\t" \
        "v_mov_b64 v[156:157], 0\n\t" \
        "v_mov_b64 v[158:159], 0\n\t" \
        "v_mov_b64 v[160:161], 0\n\t" \
        "v_mov_b64 v[162:163], 0\n\t" \
        "v_mov_b64 v[164:165], 0\n\t" \
        "v_mov_b64 v[166:167], 0\n\t" \
        "v_mov_b64 v[168:169], 0\n\t" \
        "v_mov_b64 v[170:171], 0\n\t" \
        "v_mov_b64 v[172:173], 0\n\t" \
        "v_mov_b64 v[174:175], 0\n\t" \
        "Lp2%=:\n\t" \
        "s_memtime s[92:93]\n\t" \
        "s_waitcnt lgkmcnt(0)\n\t" \
        "s_sub_u32 s73, s92, s63\n\t" \
        "s_movk_i32 s65, 4096\n\t" \
        "La2%=:\n\t" \
        "s_add_u32 s62, s62, 1152\n\t" \
        "s_sub_u32 s65, s65, 1\n\t" \
        "s_cbranch_scc1 La2%=x\n\t" \
        "s_sub_u32 s64, s62, s73\n\t" \
        "s_cmp_lt_i32 s64, -200\n\t" \
        "s_cbranch_scc1 La2%=\n\t" \
        "La2%=x:\n\t" \
        "s_branch Lw2%=\n\t" \
        "Lend%=:\n\t" \
        "s_waitcnt vmcnt(0) lgkmcnt(0)\n\t"
#define S_ASM_CLOB \
        "v32", "v33", "v34", "v35", "v36", "v37", "v38", "v39", "v40", "v41", "v42", "v43", "v44", "v45", "v46", "v47", "v48", "v49", "v50", "v51", "v52", "v53", "v54", "v55", \
        "v56", "v57", "v58", "v59", "v60", "v61", "v62", "v63", "v64", "v65", "v66", "v67", "v68", "v69", "v70", "v71", "v72", "v73", "v74", "v75", "v76", "v77", "v78", "v79", \
        "v80", "v81", "v82", "v83", "v84", "v85", "v86", "v87", "v88", "v89", "v90", "v91", "v92", "v93", "v94", "v95", "v96", "v97", "v98", "v99", "v100", "v101", "v102", "v103", \
        "v104", "v105", "v106", "v107", "v108", "v109", "v110", "v111", "v112", "v113", "v114", "v115", "v116", "v117", "v118", "v119", "v120", "v121", "v122", "v123", "v124", "v125", "v126", "v127", \
        "v128", "v129", "v130", "v131", "v132", "v133", "v134", "v135", "v136", "v137", "v138", "v139", "v140", "v141", "v142", "v143", "v144", "v145", "v146", "v147", "v148", "v149", "v150", "v151", \
        "v152", "v153", "v154", "v155", "v156", "v157", "v158", "v159", "v160", "v161", "v162", "v163", "v164", "v165", "v166", "v167", "v168", "v169", "v170", "v171", "v172", "v173", "v174", "v175", \
        "v176", "v177", "v178", "v179", "v180", "v181", "v182", "v183", "v184", "v185", "v186", "v187", "v188", "v189", "v190", "v191", "v192", "v193", "v194", "v195", "v196", "v197", "v198", "v199", \
        "v200", "v201", "v202", "v203", "v204", "v205", "v206", "v207", "v208", "v209", "v210", "v211", "v212", "v213", "v214", "v215", "v216", "v217", "v218", "v219", "v220", "v221", "v222", "v223", \
        "v224", "v225", "v226", "v227", "v228", "v229", "v230", "v231", "v232", "v233", "v234", "v235", "v236", "v237", "v238", "v239", "v240", "v241", "v242", "v243", "v244", "v245", "v246", "v247", \
        "v248", "v249", "v250", "v251", "v252", "v253", "v254", "v255", "a0", "a1", "a2", "a3", "a4", "a5", "a6", "a7", "a8", "a9", "a10", "a11", "a12", "a13", "a14", "a15", \
        "a16", "a17", "a18", "a19", "a20", "a21", "a22", "a23", "a24", "a25", "a26", "a27", "a28", "a29", "a30", "a31", "a32", "a33", "a34", "a35", "a36", "a37", "a38", "a39", \
        "a40", "a41", "a42", "a43", "a44", "a45", "a46", "a47", "a48", "a49", "a50", "a51", "a52", "a53", "a54", "a55", "a56", "a57", "a58", "a59", "a60", "a61", "a62", "a63", \
        "a64", "a65", "a66", "a67", "a68", "a69", "a70", "a71", "a72", "a73", "a74", "a75", "a76", "a77", "a78", "a79", "a80", "a81", "a82", "a83", "a84", "a85", "a86", "a87", \
        "a88", "a89", "a90", "a91", "a92", "a93", "a94", "a95", "a96", "a97", "a98", "a99", "a100", "a101", "a102", "a103", "a104", "a105", "a106", "a107", "a108", "a109", "a110", "a111", \
        "a112", "a113", "a114", "a115", "a116", "a117", "a118", "a119", "a120", "a121", "a122", "a123", "a124", "a125", "a126", "a127", "a128", "a129", "a130", "a131", "a132", "a133", "a134", "a135", \
        "a136", "a137", "a138", "a139", "a140", "a141", "a142", "a143", "a144", "a145", "a146", "a147", "a148", "a149", "a150", "a151", "a152", "a153", "a154", "a155", "a156", "a157", "a158", "a159", \
        "a160", "a161", "a162", "a163", "a164", "a165", "a166", "a167", "a168", "a169", "a170", "a171", "a172", "a173", "a174", "a175", "a176", "a177", "a178", "a179", "a180", "a181", "a182", "a183", \
        "a184", "a185", "a186", "a187", "a188", "a189", "a190", "a191", "a192", "a193", "a194", "a195", "a196", "a197", "a198", "a199", "a200", "a201", "a202", "a203", "a204", "a205", "a206", "a207", \
        "a208", "a209", "a210", "a211", "a212", "a213", "a214", "a215", "a216", "a217", "a218", "a219", "a220", "a221", "a222", "a223", "a224", "a225", "a226", "a227", "a228", "a229", "a230", "a231", \
        "a232", "a233", "a234", "a235", "a236", "a237", "a238", "a239", "a240", "a241", "a242", "a243", "a244", "a245", "a246", "a247", "a248", "a249", "a250", "a251", "a252", "a253", "a254", "a255", \
        "s33", "s34", "s35", "s36", "s37", "s38", "s39", "s40", "s41", "s42", "s43", "s44", "s45", "s46", "s47", "s48", "s49", "s50", "s51", "s52", "s53", "s54", "s55", "s56", \
        "s57", "s58", "s59", "s60", "s61", "s62", "s63", "s64", "s65", "s66", "s67", "s68", "s69", "s70", "s71", "s72", "s73", "s74", "s75", "s76", "s77", "s78", "s79", "s80", \
        "s81", "s82", "s83", "s84", "s85", "s86", "s87", "s88", "s89", "s90", "s91", "s92", "s93", "s94", "s95", "s96", "s97", "s98", "s99", "vcc", "scc", "memory"
__device__ __forceinline__ void s_role(const Params& P, int h, unsigned lb) {
    const int tid = threadIdx.x, lane = tid & 63, w = __builtin_amdgcn_readfirstlane(tid >> 6);
    const rsrc_t rs = ws_rsrc(P);
    constexpr unsigned L_IMG = 73728, L_XCH = L_IMG + 4 * 2560, L_SCH = L_XCH + 6144, L_DBG = L_SCH + 64;
    const unsigned wbase = OFF_AW + (h * 4 + w) * 49u * 2048u;
    const unsigned img = lb + L_IMG + w * 2560;
    for (int u = lane; u < 640; u += 64) *LDSP(unsigned, img + u * 4) = 0u;
    if (tid == 0) *LDSP(u2, lb + L_SCH) = (u2){0u, 0u};
    const int rho = lane & 15, qa = lane >> 4;
    unsigned vo_mem[7];
#pragma unroll
    for (int u = 0; u < 7; ++u) vo_mem[u] = ((7 * w + 4 * u + qa) % 28) * 256 + rho * 16;
    const int idx = (rho & 2) ? 0xEEEE : 0x4444;
    const unsigned a_wr = img + rho * 40 + qa * 8;
    unsigned a_wl[3];
#pragma unroll
    for (int x = 0; x < 3; ++x) { int set, im, row, hf; a_slot(x == 2 ? 6 : x + 2, rho, set, im, row, hf); a_wl[x] = img + set * 1280 + 640 + row * 40 + qa * 8 + hf * 4; }
    const unsigned a_tr = img + (4 * qa + (rho >> 2)) * 40 + (lane & 3) * 8;
    const unsigned a_x = lb + L_XCH + (w == 3 ? 1024 : 0) + lane * 16;
    const unsigned vo_px = 32768 + h * 512 + (rho >> 3) * 256 + (8 * (w & 1) + 4 * (qa >> 1) + 2 * ((rho >> 2) & 1) + (qa & 1)) * 16 + (rho & 3) * 4;
    const unsigned vo_pm = 65536 + h * 128 + (w & 1) * 64 + qa * 16 + (rho >> 2) * 4;
    const unsigned vo_pr = 65536 + 4096 + ((lane & 60) == 28 ? 0 : lane) * 64;
    const unsigned tb = chunk_table(0u, lane), tsh = 2 * (lane / 20);
    WAIT_A(h);
    TL(1, 2)
    asm volatile(S_ASM_TEXT
        :
        : [rs] "s"(rs), [wb] "s"(wbase), [hm] "s"(0xCCCCCCCCCCCCCCCCull), [w] "s"(w), [so] "s"(65536 + 6144 + (h * 2 + (w & 1)) * 64), [gsc] TUN(P.KS, 105), [dec] "i"(4), [ks0] TUN(P.mask, 2700), [l0] "i"(500),
          [oxk] "i"(OFF_XK), [opr] "i"(CT_INIT),
          [m0] "v"(vo_mem[0]), [m1] "v"(vo_mem[1]), [m2] "v"(vo_mem[2]), [m3] "v"(vo_mem[3]), [m4] "v"(vo_mem[4]), [m5] "v"(vo_mem[5]), [m6] "v"(vo_mem[6]),
          [idx] "v"(idx), [lw] "v"(lb + w * 18432 + lane * 16), [awr] "v"(a_wr), [wl0] "v"(a_wl[0]), [wl1] "v"(a_wl[1]), [wl2] "v"(a_wl[2]),
          [atr] "v"(a_tr), [ax] "v"(a_x), [vpx] "v"(vo_px), [vpm] "v"(vo_pm), [vpr] "v"(vo_pr), [l16] "v"(lane * 16), [asch] "v"(lb + L_SCH),
          [tb] "v"(tb), [tsh] "v"(tsh)

        : S_ASM_CLOB);
    TL(1, 1)
}

#define ZSLEEP 5
#define C_ASM_TEXT \
        "s_getpc_b64 s[92:93]\n\t" \
        "LCbeg_%=:\n\t" \
        "v_lshlrev_b32 v204, 3, %[l16]\n\t" \
        "v_min_u32 v204, LCend_%=-LCbeg_%=, v204\n\t" \
        "global_load_dword v205, v204, s[92:93]\n\t" \
        "v_mov_b32 v209, %[tb]\n\t" \
        "s_mov_b32 s60, 0\n\t" \
        "s_cmp_eq_u32 %[w], 0\n\t" \
        "s_cbranch_scc1 LCmain_%=\n\t" \
        "s_add_u32 s64, %[wb], 0\n\t" \
        "buffer_load_dwordx4 a[0:3], %[l16], %[rs], s64 offen offset:0 sc1\n\t" \
        "buffer_load_dwordx4 a[4:7], %[l16], %[rs], s64 offen offset:1024 sc1\n\t" \
        "s_add_u32 s64, %[wb], 2048\n\t" \
        "buffer_load_dwordx4 a[8:11], %[l16], %[rs], s64 offen offset:0 sc1\n\t" \
        "buffer_load_dwordx4 a[12:15], %[l16], %[rs], s64 offen offset:1024 sc1\n\t" \
        "s_add_u32 s64, %[wb], 4096\n\t" \
        "buffer_load_dwordx4 a[16:19], %[l16], %[rs], s64 offen offset:0 sc1\n\t" \
        "buffer_load_dwordx4 a[20:23], %[l16], %[rs], s64 offen offset:1024 sc1\n\t" \
        "s_add_u32 s64, %[wb], 6144\n\t" \
        "buffer_load_dwordx4 a[24:27], %[l16], %[rs], s64 offen offset:0 sc1\n\t" \
        "buffer_load_dwordx4 a[28:31], %[l16], %[rs], s64 offen offset:1024 sc1\n\t" \
        "s_add_u32 s64, %[wb], 8192\n\t" \
        "buffer_load_dwordx4 a[32:35], %[l16], %[rs], s64 offen offset:0 sc1\n\t" \
        "buffer_load_dwordx4 a[36:39], %[l16], %[rs], s64 offen offset:1024 sc1\n\t" \
        "s_add_u32 s64, %[wb], 10240\n\t" \
        "buffer_load_dwordx4 a[40:43], %[l16], %[rs], s64 offen offset:0 sc1\n\t" \
        "buffer_load_dwordx4 a[44:47], %[l16], %[rs], s64 offen offset:1024 sc1\n\t" \
        "s_add_u32 s64, %[wb], 12288\n\t" \
        "buffer_load_dwordx4 a[48:51], %[l16], %[rs], s64 offen offset:0 sc1\n\t" \
        "buffer_load_dwordx4 a[52:55], %[l16], %[rs], s64 offen offset:1024 sc1\n\t" \
        "s_add_u32 s64, %[wb], 14336\n\t" \
        "buffer_load_dwordx4 a[56:59], %[l16], %[rs], s64 offen offset:0 sc1\n\t" \
        "buffer_load_dwordx4 a[60:63], %[l16], %[rs], s64 offen offset:1024 sc1\n\t" \
        "s_add_u32 s64, %[wb], 16384\n\t" \
        "buffer_load_dwordx4 a[64:67], %[l16], %[rs], s64 offen offset:0 sc1\n\t" \
        "buffer_load_dwordx4 a[68:71], %[l16], %[rs], s64 offen offset:1024 sc1\n\t" \
        "s_add_u32 s64, %[wb], 18432\n\t" \
        "buffer_load_dwordx4 a[72:75], %[l16], %[rs], s64 offen offset:0 sc1\n\t" \
        "buffer_load_dwordx4 a[76:79], %[l16], %[rs], s64 offen offset:1024 sc1\n\t" \
        "s_add_u32 s64, %[wb], 20480\n\t" \
        "buffer_load_dwordx4 a[80:83], %[l16], %[rs], s64 offen offset:0 sc1\n\t" \
        "buffer_load_dwordx4 a[84:87], %[l16], %[rs], s64 offen offset:1024 sc1\n\t" \
        "s_add_u32 s64, %[wb], 22528\n\t" \
        "buffer_load_dwordx4 a[88:91], %[l16], %[rs], s64 offen offset:0 sc1\n\t" \
        "buffer_load_dwordx4 a[92:95], %[l16], %[rs], s64 offen offset:1024 sc1\n\t" \
        "s_add_u32 s64, %[wb], 24576\n\t" \
        "buffer_load_dwordx4 a[96:99], %[l16], %[rs], s64 offen offset:0 sc1\n\t" \
        "buffer_load_dwordx4 a[100:103], %[l16], %[rs], s64 offen offset:1024 sc1\n\t" \
        "s_add_u32 s64, %[wb], 26624\n\t" \
        "buffer_load_dwordx4 a[104:107], %[l16], %[rs], s64 offen offset:0 sc1\n\t" \
        "buffer_load_dwordx4 a[108:111], %[l16], %[rs], s64 offen offset:1024 sc1\n\t" \
        "s_add_u32 s64, %[wb], 28672\n\t" \
        "buffer_load_dwordx4 a[112:115], %[l16], %[rs], s64 offen offset:0 sc1\n\t" \
        "buffer_load_dwordx4 a[116:119], %[l16], %[rs], s64 offen offset:1024 sc1\n\t" \
        "s_add_u32 s64, %[wb], 30720\n\t" \
        "buffer_load_dwordx4 a[120:123], %[l16], %[rs], s64 offen offset:0 sc1\n\t" \
        "buffer_load_dwordx4 a[124:127], %[l16], %[rs], s64 offen offset:1024 sc1\n\t" \
        "s_add_u32 s64, %[wb], 32768\n\t" \
        "buffer_load_dwordx4 a[128:131], %[l16], %[rs], s64 offen offset:0 sc1\n\t" \
        "buffer_load_dwordx4 a[132:135], %[l16], %[rs], s64 offen offset:1024 sc1\n\t" \
        "s_add_u32 s64, %[wb], 34816\n\t" \
        "buffer_load_dwordx4 a[136:139], %[l16], %[rs], s64 offen offset:0 sc1\n\t" \
        "buffer_load_dwordx4 a[140:143], %[l16], %[rs], s64 offen offset:1024 sc1\n\t" \
        "s_add_u32 s64, %[wb], 36864\n\t" \
        "buffer_load_dwordx4 a[144:147], %[l16], %[rs], s64 offen offset:0 sc1\n\t" \
        "buffer_load_dwordx4 a[148:151], %[l16], %[rs], s64 offen offset:1024 sc1\n\t" \
        "s_add_u32 s64, %[wb], 38912\n\t" \
        "buffer_load_dwordx4 a[152:155], %[l16], %[rs], s64 offen offset:0 sc1\n\t" \
        "buffer_load_dwordx4 a[156:159], %[l16], %[rs], s64 offen offset:1024 sc1\n\t" \
        "s_add_u32 s64, %[wb], 40960\n\t" \
        "buffer_load_dwordx4 a[160:163], %[l16], %[rs], s64 offen offset:0 sc1\n\t" \
        "buffer_load_dwordx4 a[164:167], %[l16], %[rs], s64 offen offset:1024 sc1\n\t" \
        "s_add_u32 s64, %[wb], 43008\n\t" \
        "buffer_load_dwordx4 a[168:171], %[l16], %[rs], s64 offen offset:0 sc1\n\t" \
        "buffer_load_dwordx4 a[172:175], %[l16], %[rs], s64 offen offset:1024 sc1\n\t" \
        "s_add_u32 s64, %[wb], 45056\n\t" \
        "buffer_load_dwordx4 a[176:179], %[l16], %[rs], s64 offen offset:0 sc1\n\t" \
        "buffer_load_dwordx4 a[180:183], %[l16], %[rs], s64 offen offset:1024 sc1\n\t" \
        "s_add_u32 s64, %[wb], 47104\n\t" \
        "buffer_load_dwordx4 a[184:187], %[l16], %[rs], s64 offen offset:0 sc1\n\t" \
        "buffer_load_dwordx4 a[188:191], %[l16], %[rs], s64 offen offset:1024 sc1\n\t" \
        "s_add_u32 s64, %[wb], 49152\n\t" \
        "buffer_load_dwordx4 a[192:195], %[l16], %[rs], s64 offen offset:0 sc1\n\t" \
        "buffer_load_dwordx4 a[196:199], %[l16], %[rs], s64 offen offset:1024 sc1\n\t" \
        "s_add_u32 s64, %[wb], 51200\n\t" \
        "buffer_load_dwordx4 a[200:203], %[l16], %[rs], s64 offen offset:0 sc1\n\t" \
        "buffer_load_dwordx4 a[204:207], %[l16], %[rs], s64 offen offset:1024 sc1\n\t" \
        "s_add_u32 s64, %[wb], 53248\n\t" \
        "buffer_load_dwordx4 a[208:211], %[l16], %[rs], s64 offen offset:0 sc1\n\t" \
        "buffer_load_dwordx4 a[212:215], %[l16], %[rs], s64 offen offset:1024 sc1\n\t" \
        "s_add_u32 s64, %[wb], 55296\n\t" \
        "buffer_load_dwordx4 a[216:219], %[l16], %[rs], s64 offen offset:0 sc1\n\t" \
        "buffer_load_dwordx4 a[220:223], %[l16], %[rs], s64 offen offset:1024 sc1\n\t" \
        "s_add_u32 s64, %[wb], 57344\n\t" \
        "buffer_load_dwordx4 a[224:227], %[l16], %[rs], s64 offen offset:0 sc1\n\t" \
        "buffer_load_dwordx4 a[228:231], %[l16], %[rs], s64 offen offset:1024 sc1\n\t" \
        "s_waitcnt vmcnt(56)\n\t" \
        "s_add_u32 s64, %[wb], 59392\n\t" \
        "buffer_load_dwordx4 a[232:235], %[l16], %[rs], s64 offen offset:0 sc1\n\t" \
        "buffer_load_dwordx4 a[236:239], %[l16], %[rs], s64 offen offset:1024 sc1\n\t" \
        "s_waitcnt vmcnt(56)\n\t" \
        "s_add_u32 s64, %[wb], 61440\n\t" \
        "buffer_load_dwordx4 a[240:243], %[l16], %[rs], s64 offen offset:0 sc1\n\t" \
        "buffer_load_dwordx4 a[244:247], %[l16], %[rs], s64 offen offset:1024 sc1\n\t" \
        "s_waitcnt vmcnt(56)\n\t" \
        "s_add_u32 s64, %[wb], 63488\n\t" \
        "buffer_load_dwordx4 a[248:251], %[l16], %[rs], s64 offen offset:0 sc1\n\t" \
        "buffer_load_dwordx4 a[252:255], %[l16], %[rs], s64 offen offset:1024 sc1\n\t" \
        "s_waitcnt vmcnt(56)\n\t" \
        "s_add_u32 s64, %[wb], 65536\n\t" \
        "buffer_load_dwordx4 v[32:35], %[l16], %[rs], s64 offen offset:0 sc1\n\t" \
        "buffer_load_dwordx4 v[36:39], %[l16], %[rs], s64 offen offset:1024 sc1\n\t" \
        "s_waitcnt vmcnt(56)\n\t" \
        "s_add_u32 s64, %[wb], 67584\n\t" \
        "buffer_load_dwordx4 v[40:43], %[l16], %[rs], s64 offen offset:0 sc1\n\t" \
        "buffer_load_dwordx4 v[44:47], %[l16], %[rs], s64 offen offset:1024 sc1\n\t" \
        "s_waitcnt vmcnt(56)\n\t" \
        "s_add_u32 s64, %[wb], 69632\n\t" \
        "buffer_load_dwordx4 v[48:51], %[l16], %[rs], s64 offen offset:0 sc1\n\t" \
        "buffer_load_dwordx4 v[52:55], %[l16], %[rs], s64 offen offset:1024 sc1\n\t" \
        "s_waitcnt vmcnt(56)\n\t" \
        "s_add_u32 s64, %[wb], 71680\n\t" \
        "buffer_load_dwordx4 v[56:59], %[l16], %[rs], s64 offen offset:0 sc1\n\t" \
        "buffer_load_dwordx4 v[60:63], %[l16], %[rs], s64 offen offset:1024 sc1\n\t" \
        "s_waitcnt vmcnt(56)\n\t" \
        "s_add_u32 s64, %[wb], 73728\n\t" \
        "buffer_load_dwordx4 v[64:67], %[l16], %[rs], s64 offen offset:0 sc1\n\t" \
        "buffer_load_dwordx4 v[68:71], %[l16], %[rs], s64 offen offset:1024 sc1\n\t" \
        "s_waitcnt vmcnt(56)\n\t" \
        "s_add_u32 s64, %[wb], 75776\n\t" \
        "buffer_load_dwordx4 v[72:75], %[l16], %[rs], s64 offen offset:0 sc1\n\t" \
        "buffer_load_dwordx4 v[76:79], %[l16], %[rs], s64 offen offset:1024 sc1\n\t" \
        "s_waitcnt vmcnt(56)\n\t" \
        "s_add_u32 s64, %[wb], 77824\n\t" \
        "buffer_load_dwordx4 v[80:83], %[l16], %[rs], s64 offen offset:0 sc1\n\t" \
        "buffer_load_dwordx4 v[84:87], %[l16], %[rs], s64 offen offset:1024 sc1\n\t" \
        "s_waitcnt vmcnt(56)\n\t" \
        "s_add_u32 s64, %[wb], 79872\n\t" \
        "buffer_load_dwordx4 v[88:91], %[l16], %[rs], s64 offen offset:0 sc1\n\t" \
        "buffer_load_dwordx4 v[92:95], %[l16], %[rs], s64 offen offset:1024 sc1\n\t" \
        "s_waitcnt vmcnt(56)\n\t" \
        "s_add_u32 s64, %[wb], 81920\n\t" \
        "buffer_load_dwordx4 v[96:99], %[l16], %[rs], s64 offen offset:0 sc1\n\t" \
        "buffer_load_dwordx4 v[100:103], %[l16], %[rs], s64 offen offset:1024 sc1\n\t" \
        "s_waitcnt vmcnt(56)\n\t" \
        "s_add_u32 s64, %[wb], 83968\n\t" \
        "buffer_load_dwordx4 v[104:107], %[l16], %[rs], s64 offen offset:0 sc1\n\t" \
        "buffer_load_dwordx4 v[108:111], %[l16], %[rs], s64 offen offset:1024 sc1\n\t" \
        "s_waitcnt vmcnt(56)\n\t" \
        "s_waitcnt vmcnt(0)\n\t" \
        "s_barrier\n\t" \
        "ds_read_b32 v209, %[lz] offset:8192\n\t" \
        "s_waitcnt lgkmcnt(0)\n\t" \
        "LCs1_%=:\n\t" \
        "v_readlane_b32 s61, v209, s60\n\t" \
        "s_add_u32 s72, s60, 1\n\t" \
        "s_mov_b32 s66, 0\n\t" \
        "s_add_u32 s69, s61, 4096\n\t" \
        "s_nop 3\n\t" \
        "buffer_load_dwordx4 v[152:155], %[l16], %[rs], s61 offen offset:0 sc1\n\t" \
        "s_nop 8\n\t" \
        "buffer_load_dwordx4 v[156:159], %[l16], %[rs], s61 offen offset:1024 sc1\n\t" \
        "s_nop 8\n\t" \
        "buffer_load_dwordx4 v[160:163], %[l16], %[rs], s61 offen offset:2048 sc1\n\t" \
        "s_nop 8\n\t" \
        "buffer_load_dwordx4 v[164:167], %[l16], %[rs], s61 offen offset:3072 sc1\n\t" \
        "s_nop 8\n\t" \
        "buffer_load_dwordx4 v[168:171], %[l16], %[rs], s69 offen offset:0 sc1\n\t" \
        "s_nop 8\n\t" \
        "buffer_load_dwordx4 v[172:175], %[l16], %[rs], s69 offen offset:1024 sc1\n\t" \
        "s_nop 8\n\t" \
        "buffer_load_dwordx4 v[176:179], %[l16], %[rs], s69 offen offset:2048 sc1\n\t" \
        "LCm1_%=:\n\t" \
        "v_mov_b64 v[180:181], 0\n\t" \
        "v_mov_b64 v[182:183], 0\n\t" \
        "v_mov_b64 v[184:185], 0\n\t" \
        "v_mov_b64 v[186:187], 0\n\t" \
        "v_mov_b64 v[188:189], 0\n\t" \
        "v_mov_b64 v[190:191], 0\n\t" \
        "v_mov_b64 v[192:193], 0\n\t" \
        "v_mov_b64 v[194:195], 0\n\t" \
        "v_mov_b64 v[196:197], 0\n\t" \
        "v_mov_b64 v[198:199], 0\n\t" \
        "v_mov_b64 v[200:201], 0\n\t" \
        "v_mov_b64 v[202:203], 0\n\t" \
        "s_waitcnt vmcnt(6)\n\t" \
        "v_smfmac_f32_16x16x64_bf16 v[180:183], v[152:155], a[0:7], %[idx]\n\t" \
        "v_smfmac_f32_16x16x64_bf16 v[184:187], v[152:155], a[56:63], %[idx]\n\t" \
        "v_smfmac_f32_16x16x64_bf16 v[188:191], v[152:155], a[112:119], %[idx]\n\t" \
        "v_smfmac_f32_16x16x64_bf16 v[192:195], v[152:155], a[168:175], %[idx]\n\t" \
        "v_smfmac_f32_16x16x64_bf16 v[196:199], v[152:155], a[224:231], %[idx]\n\t" \
        "v_smfmac_f32_16x16x64_bf16 v[200:203], v[152:155], v[56:63], %[idx]\n\t" \
        "s_waitcnt vmcnt(5)\n\t" \
        "v_smfmac_f32_16x16x64_bf16 v[180:183], v[156:159], a[8:15], %[idx]\n\t" \
        "v_smfmac_f32_16x16x64_bf16 v[184:187], v[156:159], a[64:71], %[idx]\n\t" \
        "v_smfmac_f32_16x16x64_bf16 v[188:191], v[156:159], a[120:127], %[idx]\n\t" \
        "v_smfmac_f32_16x16x64_bf16 v[192:195], v[156:159], a[176:183], %[idx]\n\t" \
        "v_smfmac_f32_16x16x64_bf16 v[196:199], v[156:159], a[232:239], %[idx]\n\t" \
        "v_smfmac_f32_16x16x64_bf16 v[200:203], v[156:159], v[64:71], %[idx]\n\t" \
        "s_waitcnt vmcnt(4)\n\t" \
        "v_smfmac_f32_16x16x64_bf16 v[180:183], v[160:163], a[16:23], %[idx]\n\t" \
        "v_smfmac_f32_16x16x64_bf16 v[184:187], v[160:163], a[72:79], %[idx]\n\t" \
        "v_smfmac_f32_16x16x64_bf16 v[188:191], v[160:163], a[128:135], %[idx]\n\t" \
        "v_smfmac_f32_16x16x64_bf16 v[192:195], v[160:163], a[184:191], %[idx]\n\t" \
        "v_smfmac_f32_16x16x64_bf16 v[196:199], v[160:163], a[240:247], %[idx]\n\t" \
        "v_smfmac_f32_16x16x64_bf16 v[200:203], v[160:163], v[72:79], %[idx]\n\t" \
        "s_waitcnt vmcnt(3)\n\t" \
        "v_smfmac_f32_16x16x64_bf16 v[180:183], v[164:167], a[24:31], %[idx]\n\t" \
        "v_smfmac_f32_16x16x64_bf16 v[184:187], v[164:167], a[80:87], %[idx]\n\t" \
        "v_smfmac_f32_16x16x64_bf16 v[188:191], v[164:167], a[136:143], %[idx]\n\t" \
        "v_smfmac_f32_16x16x64_bf16 v[192:195], v[164:167], a[192:199], %[idx]\n\t" \
        "v_smfmac_f32_16x16x64_bf16 v[196:199], v[164:167], a[248:255], %[idx]\n\t" \
        "v_smfmac_f32_16x16x64_bf16 v[200:203], v[164:167], v[80:87], %[idx]\n\t" \
        "s_waitcnt vmcnt(2)\n\t" \
        "v_smfmac_f32_16x16x64_bf16 v[180:183], v[168:171], a[32:39], %[idx]\n\t" \
        "v_smfmac_f32_16x16x64_bf16 v[184:187], v[168:171], a[88:95], %[idx]\n\t" \
        "v_smfmac_f32_16x16x64_bf16 v[188:191], v[168:171], a[144:151], %[idx]\n\t" \
        "v_smfmac_f32_16x16x64_bf16 v[192:195], v[168:171], a[200:207], %[idx]\n\t" \
        "v_smfmac_f32_16x16x64_bf16 v[196:199], v[168:171], v[32:39], %[idx]\n\t" \
        "v_smfmac_f32_16x16x64_bf16 v[200:203], v[168:171], v[88:95], %[idx]\n\t" \
        "s_waitcnt vmcnt(1)\n\t" \
        "v_smfmac_f32_16x16x64_bf16 v[180:183], v[172:175], a[40:47], %[idx]\n\t" \
        "v_smfmac_f32_16x16x64_bf16 v[184:187], v[172:175], a[96:103], %[idx]\n\t" \
        "v_smfmac_f32_16x16x64_bf16 v[188:191], v[172:175], a[152:159], %[idx]\n\t" \
        "v_smfmac_f32_16x16x64_bf16 v[192:195], v[172:175], a[208:215], %[idx]\n\t" \
        "v_smfmac_f32_16x16x64_bf16 v[196:199], v[172:175], v[40:47], %[idx]\n\t" \
        "v_smfmac_f32_16x16x64_bf16 v[200:203], v[172:175], v[96:103], %[idx]\n\t" \
        "s_waitcnt vmcnt(0)\n\t" \
        "v_smfmac_f32_16x16x64_bf16 v[180:183], v[176:179], a[48:55], %[idx]\n\t" \
        "v_smfmac_f32_16x16x64_bf16 v[184:187], v[176:179], a[104:111], %[idx]\n\t" \
        "v_smfmac_f32_16x16x64_bf16 v[188:191], v[176:179], a[160:167], %[idx]\n\t" \
        "v_smfmac_f32_16x16x64_bf16 v[192:195], v[176:179], a[216:223], %[idx]\n\t" \
        "v_smfmac_f32_16x16x64_bf16 v[196:199], v[176:179], v[48:55], %[idx]\n\t" \
        "v_smfmac_f32_16x16x64_bf16 v[200:203], v[176:179], v[104:111], %[idx]\n\t" \
        "v_add_f32 v204, v180, v182\n\t" \
        "v_add_f32 v205, v181, v183\n\t" \
        "v_cmp_u_f32 s[70:71], v204, v205\n\t" \
        "v_cvt_pk_bf16_f32 v180, v204, v205\n\t" \
        "ds_write_b32 %[azw], v180 offset:0\n\t" \
        "v_add_f32 v204, v184, v186\n\t" \
        "v_add_f32 v205, v185, v187\n\t" \
        "v_cvt_pk_bf16_f32 v184, v204, v205\n\t" \
        "ds_write_b32 %[azv], v184 offset:0\n\t" \
        "v_add_f32 v204, v188, v190\n\t" \
        "v_add_f32 v205, v189, v191\n\t" \
        "v_cvt_pk_bf16_f32 v188, v204, v205\n\t" \
        "ds_write_b32 %[azw], v188 offset:512\n\t" \
        "v_add_f32 v204, v192, v194\n\t" \
        "v_add_f32 v205, v193, v195\n\t" \
        "v_cvt_pk_bf16_f32 v192, v204, v205\n\t" \
        "ds_write_b32 %[azv], v192 offset:512\n\t" \
        "v_add_f32 v204, v196, v198\n\t" \
        "v_add_f32 v205, v197, v199\n\t" \
        "v_cvt_pk_bf16_f32 v196, v204, v205\n\t" \
        "ds_write_b32 %[azw], v196 offset:1024\n\t" \
        "v_add_f32 v204, v200, v202\n\t" \
        "v_add_f32 v205, v201, v203\n\t" \
        "v_cvt_pk_bf16_f32 v200, v204, v205\n\t" \
        "ds_write_b32 %[azv], v200 offset:1024\n\t" \
        "s_cmp_lg_u64 s[70:71], 0\n\t" \
        "s_cbranch_scc1 LCn1_%=\n\t" \
        "LCc1_%=:\n\t" \
        "s_waitcnt lgkmcnt(0)\n\t" \
        "s_barrier\n\t" \
        "s_barrier\n\t" \
        "s_sleep 4\n\t" \
        "s_mov_b32 s60, s72\n\t" \
        "s_cmp_lt_u32 s60, 63\n\t" \
        "s_cbranch_scc1 LCs1_%=\n\t" \
        "s_branch LCend_%=\n\t" \
        "LCn1_%=:\n\t" \
        "s_cmp_lg_u32 s66, 0\n\t" \
        "s_cbranch_scc1 LCc1_%=\n\t" \
        "s_mov_b32 s66, 1\n\t" \
        "s_movk_i32 s65, 16384\n\t" \
        "LCpl1_%=:\n\t" \
        "buffer_load_dwordx4 v[152:155], %[l16], %[rs], s61 offen offset:0 sc1\n\t" \
        "buffer_load_dwordx4 v[156:159], %[l16], %[rs], s61 offen offset:1024 sc1\n\t" \
        "buffer_load_dwordx4 v[160:163], %[l16], %[rs], s61 offen offset:2048 sc1\n\t" \
        "buffer_load_dwordx4 v[164:167], %[l16], %[rs], s61 offen offset:3072 sc1\n\t" \
        "buffer_load_dwordx4 v[168:171], %[l16], %[rs], s69 offen offset:0 sc1\n\t" \
        "buffer_load_dwordx4 v[172:175], %[l16], %[rs], s69 offen offset:1024 sc1\n\t" \
        "buffer_load_dwordx4 v[176:179], %[l16], %[rs], s69 offen offset:2048 sc1\n\t" \
        "s_waitcnt vmcnt(0)\n\t" \
        "v_max_u32 v204, v152, v153\n\t" \
        "v_max3_u32 v204, v204, v154, v155\n\t" \
        "v_max3_u32 v204, v204, v156, v157\n\t" \
        "v_max3_u32 v204, v204, v158, v159\n\t" \
        "v_max3_u32 v204, v204, v160, v161\n\t" \
        "v_max3_u32 v204, v204, v162, v163\n\t" \
        "v_max3_u32 v204, v204, v164, v165\n\t" \
        "v_max3_u32 v204, v204, v166, v167\n\t" \
        "v_max3_u32 v204, v204, v168, v169\n\t" \
        "v_max3_u32 v204, v204, v170, v171\n\t" \
        "v_max3_u32 v204, v204, v172, v173\n\t" \
        "v_max3_u32 v204, v204, v174, v175\n\t" \
        "v_max3_u32 v204, v204, v176, v177\n\t" \
        "v_max3_u32 v204, v204, v178, v179\n\t" \
        "v_cmp_eq_u32 vcc, -1, v204\n\t" \
        "s_cbranch_vccz LCm1_%=\n\t" \
        "s_sub_u32 s65, s65, 1\n\t" \
        "s_cbranch_scc0 LCpl1_%=\n\t" \
        "s_branch LCm1_%=\n\t" \
        "LCmain_%=:\n\t" \
        "v_lshrrev_b32 v200, 2, %[l16]\n\t" \
        "v_and_b32 v201, 0x7c, v200\n\t" \
        "s_movk_i32 s65, 16384\n\t" \
        "s_mov_b32 s64, %[opr]\n\t" \
        "LCpg_%=:\n\t" \
        "buffer_load_dword v204, v201, %[rs], s64 offen sc1\n\t" \
        "s_waitcnt vmcnt(0)\n\t" \
        "v_cmp_eq_u32 vcc, -1, v204\n\t" \
        "s_cbranch_vccz LCpgd_%=\n\t" \
        "s_sleep 4\n\t" \
        "s_sub_u32 s65, s65, 1\n\t" \
        "s_cbranch_scc0 LCpg_%=\n\t" \
        "LCpgd_%=:\n\t" \
        "v_lshrrev_b32 v204, %[tsh], v204\n\t" \
        "v_and_b32 v204, 3, v204\n\t" \
        "v_lshlrev_b32 v204, 13, v204\n\t" \
        "v_xor_b32 v209, %[tb], v204\n\t" \
        "ds_write_b32 %[lz], v209 offset:8192\n\t" \
        "s_waitcnt lgkmcnt(0)\n\t" \
        "s_mov_b32 s90, %[zo]\n\t" \
        "s_mov_b32 s91, %[omc]\n\t" \
        "s_mov_b64 s[96:97], 0\n\t" \
        "s_mov_b32 s95, 0\n\t" \
        "s_movk_i32 s38, 780\n\t" \
        "s_mov_b32 s74, 0\n\t" \
        "s_mov_b32 s75, 0\n\t" \
        "s_mov_b32 s76, 1\n\t" \
        "s_mov_b32 s77, 0\n\t" \
        "s_mov_b32 s78, 0\n\t" \
        "s_movk_i32 s65, 16384\n\t" \
        "LCya_%=:\n\t" \
        "buffer_load_dword v211, %[l4], %[rs], %[zf] offen sc1\n\t" \
        "s_waitcnt vmcnt(0)\n\t" \
        "v_cmp_ne_u32 s[96:97], -1, v211\n\t" \
        "s_nop 1\n\t" \
        "s_bitcmp1_b64 s[96:97], 0\n\t" \
        "s_cbranch_scc1 LCyb_%=\n\t" \
        "s_sleep 4\n\t" \
        "s_sub_u32 s65, s65, 1\n\t" \
        "s_cbranch_scc0 LCya_%=\n\t" \
        "LCyb_%=:\n\t" \
        "s_add_u32 s98, s90, 4096\n\t" \
        "s_add_u32 s99, s90, 8192\n\t" \
        "s_add_u32 s59, s90, 12288\n\t" \
        "buffer_load_dwordx4 v[152:155], %[l16], %[rs], s90 offen offset:0 sc1\n\t" \
        "buffer_load_dwordx4 v[156:159], %[l16], %[rs], s90 offen offset:1024 sc1\n\t" \
        "buffer_load_dwordx4 v[160:163], %[l16], %[rs], s90 offen offset:2048 sc1\n\t" \
        "buffer_load_dwordx4 v[164:167], %[l16], %[rs], s90 offen offset:3072 sc1\n\t" \
        "buffer_load_dwordx4 v[168:171], %[l16], %[rs], s98 offen offset:0 sc1\n\t" \
        "buffer_load_dwordx4 v[172:175], %[l16], %[rs], s98 offen offset:1024 sc1\n\t" \
        "buffer_load_dwordx4 v[176:179], %[l16], %[rs], s98 offen offset:2048 sc1\n\t" \
        "buffer_load_dwordx4 v[180:183], %[l16], %[rs], s98 offen offset:3072 sc1\n\t" \
        "buffer_load_dwordx4 v[184:187], %[l16], %[rs], s99 offen offset:0 sc1\n\t" \
        "buffer_load_dwordx4 v[188:191], %[l16], %[rs], s99 offen offset:1024 sc1\n\t" \
        "buffer_load_dwordx4 v[192:195], %[l16], %[rs], s99 offen offset:2048 sc1\n\t" \
        "s_barrier\n\t" \
        "s_memtime s[40:41]\n\t" \
        "s_memrealtime s[42:43]\n\t" \
        "s_nop 2\n\t" \
        "s_memrealtime s[44:45]\n\t" \
        "s_nop 2\n\t" \
        "s_memrealtime s[46:47]\n\t" \
        "s_nop 2\n\t" \
        "s_memrealtime s[48:49]\n\t" \
        "s_nop 2\n\t" \
        "s_memrealtime s[50:51]\n\t" \
        "s_nop 2\n\t" \
        "s_memrealtime s[52:53]\n\t" \
        "s_nop 2\n\t" \
        "s_memrealtime s[54:55]\n\t" \
        "s_nop 2\n\t" \
        "s_memrealtime s[56:57]\n\t" \
        "s_waitcnt lgkmcnt(0)\n\t" \
        "s_mov_b32 s41, 0\n\t" \
        "s_cmp_eq_u32 s44, s42\n\t" \
        "s_addc_u32 s41, s41, 0\n\t" \
        "s_cmp_eq_u32 s46, s42\n\t" \
        "s_addc_u32 s41, s41, 0\n\t" \
        "s_cmp_eq_u32 s48, s42\n\t" \
        "s_addc_u32 s41, s41, 0\n\t" \
        "s_cmp_eq_u32 s50, s42\n\t" \
        "s_addc_u32 s41, s41, 0\n\t" \
        "s_cmp_eq_u32 s52, s42\n\t" \
        "s_addc_u32 s41, s41, 0\n\t" \
        "s_cmp_eq_u32 s54, s42\n\t" \
        "s_addc_u32 s41, s41, 0\n\t" \
        "s_cmp_eq_u32 s56, s42\n\t" \
        "s_addc_u32 s41, s41, 0\n\t" \
        "s_lshl_b32 s41, s41, 4\n\t" \
        "s_add_u32 s40, s40, s41\n\t" \
        "s_mul_i32 s41, s42, 24\n\t" \
        "s_sub_u32 s40, s40, s41\n\t" \
        "s_sub_u32 s40, s40, 84\n\t" \
        "s_mov_b32 s39, s40\n\t" \
        "s_mov_b32 s63, s40\n\t" \
        "s_memtime s[92:93]\n\t" \
        "v_readlane_b32 s68, v209, 0\n\t" \
        "s_waitcnt lgkmcnt(0)\n\t" \
        "s_sub_u32 s67, s92, s63\n\t" \
        "s_sub_u32 s64, s67, 300\n\t" \
        "s_min_u32 s64, s64, -2\n\t" \
        "v_mov_b32 v204, s64\n\t" \
        "v_mov_b32 v205, 0x4b0\n\t" \
        "s_add_u32 s73, s68, %[po]\n\t" \
        "buffer_store_dwordx2 v[204:205], off, %[rs], s73 sc1\n\t" \
        "LCs0_%=:\n\t" \
        "v_readlane_b32 s61, v209, s60\n\t" \
        "s_add_u32 s72, s60, 1\n\t" \
        "v_readlane_b32 s68, v209, s72\n\t" \
        "s_add_u32 s98, s90, 4096\n\t" \
        "s_add_u32 s99, s90, 8192\n\t" \
        "s_add_u32 s59, s90, 12288\n\t" \
        "s_add_u32 s86, s61, 32768\n\t" \
        "s_add_u32 s87, s61, 36864\n\t" \
        "s_add_u32 s88, s61, 65536\n\t" \
        "s_sleep %[zs]\n\t" \
        "buffer_load_dwordx2 v[206:207], %[vpr], %[rs], s61 offen sc1\n\t" \
        "buffer_load_dword v211, %[l4], %[rs], %[zf] offen sc1\n\t" \
        "buffer_load_dwordx4 v[76:79], %[l16], %[rs], s99 offen offset:3072 sc1\n\t" \
        "buffer_load_dwordx4 v[80:83], %[l16], %[rs], s59 offen offset:0 sc1\n\t" \
        "buffer_load_dwordx4 v[84:87], %[l16], %[rs], s59 offen offset:1024 sc1\n\t" \
        "buffer_load_dwordx4 v[88:91], %[l16], %[rs], s59 offen offset:2048 sc1\n\t" \
        "buffer_load_dwordx4 v[92:95], %[l16], %[rs], s59 offen offset:3072 sc1\n\t" \
        "s_add_u32 s91, s91, 7168\n\t" \
        "s_add_u32 s90, s90, 458752\n\t" \
        "v_mov_b64 v[196:197], 0\n\t" \
        "v_mov_b64 v[198:199], 0\n\t" \
        "s_mov_b32 s66, 0\n\t" \
        "s_add_u32 s98, s90, 4096\n\t" \
        "s_add_u32 s99, s90, 8192\n\t" \
        "s_add_u32 s59, s90, 12288\n\t" \
        "s_sleep 3\n\t" \
        "s_memtime s[40:41]\n\t" \
        "s_memrealtime s[42:43]\n\t" \
        "s_nop 2\n\t" \
        "s_memrealtime s[44:45]\n\t" \
        "s_nop 2\n\t" \
        "s_memrealtime s[46:47]\n\t" \
        "s_nop 2\n\t" \
        "s_memrealtime s[48:49]\n\t" \
        "s_nop 2\n\t" \
        "s_memrealtime s[50:51]\n\t" \
        "s_nop 2\n\t" \
        "s_memrealtime s[52:53]\n\t" \
        "s_nop 2\n\t" \
        "s_memrealtime s[54:55]\n\t" \
        "s_nop 2\n\t" \
        "s_memrealtime s[56:57]\n\t" \
        "s_waitcnt lgkmcnt(0)\n\t" \
        "s_mov_b32 s41, 0\n\t" \
        "s_cmp_eq_u32 s44, s42\n\t" \
        "s_addc_u32 s41, s41, 0\n\t" \
        "s_cmp_eq_u32 s46, s42\n\t" \
        "s_addc_u32 s41, s41, 0\n\t" \
        "s_cmp_eq_u32 s48, s42\n\t" \
        "s_addc_u32 s41, s41, 0\n\t" \
        "s_cmp_eq_u32 s50, s42\n\t" \
        "s_addc_u32 s41, s41, 0\n\t" \
        "s_cmp_eq_u32 s52, s42\n\t" \
        "s_addc_u32 s41, s41, 0\n\t" \
        "s_cmp_eq_u32 s54, s42\n\t" \
        "s_addc_u32 s41, s41, 0\n\t" \
        "s_cmp_eq_u32 s56, s42\n\t" \
        "s_addc_u32 s41, s41, 0\n\t" \
        "s_lshl_b32 s41, s41, 4\n\t" \
        "s_add_u32 s40, s40, s41\n\t" \
        "s_mul_i32 s41, s42, 24\n\t" \
        "s_sub_u32 s40, s40, s41\n\t" \
        "s_sub_u32 s40, s40, 84\n\t" \
        "s_sub_u32 s41, s40, s63\n\t" \
        "s_max_i32 s41, s41, -352\n\t" \
        "s_min_i32 s41, s41, 0\n\t" \
        "s_mul_i32 s42, s41, 21\n\t" \
        "s_ashr_i32 s42, s42, 5\n\t" \
        "s_add_u32 s69, s40, s42\n\t" \
        "s_mul_i32 s41, s41, 14\n\t" \
        "s_ashr_i32 s41, s41, 5\n\t" \
        "s_add_u32 s39, s40, s41\n\t" \
        "s_mov_b32 s63, s40\n\t" \
        "s_waitcnt vmcnt(5)\n\t" \
        "v_cmp_ne_u32 s[96:97], -1, v211\n\t" \
        "v_mov_b64 v[32:33], v[152:153]\n\t" \
        "v_mov_b64 v[34:35], v[154:155]\n\t" \
        "v_mov_b64 v[36:37], v[156:157]\n\t" \
        "v_mov_b64 v[38:39], v[158:159]\n\t" \
        "v_mov_b64 v[40:41], v[160:161]\n\t" \
        "v_mov_b64 v[42:43], v[162:163]\n\t" \
        "v_mov_b64 v[44:45], v[164:165]\n\t" \
        "v_mov_b64 v[46:47], v[166:167]\n\t" \
        "v_mov_b64 v[48:49], v[168:169]\n\t" \
        "v_mov_b64 v[50:51], v[170:171]\n\t" \
        "v_mov_b64 v[52:53], v[172:173]\n\t" \
        "v_mov_b64 v[54:55], v[174:175]\n\t" \
        "v_mov_b64 v[56:57], v[176:177]\n\t" \
        "v_mov_b64 v[58:59], v[178:179]\n\t" \
        "v_mov_b64 v[60:61], v[180:181]\n\t" \
        "v_mov_b64 v[62:63], v[182:183]\n\t" \
        "v_mov_b64 v[64:65], v[184:185]\n\t" \
        "v_mov_b64 v[66:67], v[186:187]\n\t" \
        "v_mov_b64 v[68:69], v[188:189]\n\t" \
        "v_mov_b64 v[70:71], v[190:191]\n\t" \
        "v_mov_b64 v[72:73], v[192:193]\n\t" \
        "v_mov_b64 v[74:75], v[194:195]\n\t" \
        "s_bitcmp1_b64 s[96:97], s72\n\t" \
        "s_cbranch_scc0 LCzl_%=\n\t" \
        "LCz_%=:\n\t" \
        "s_movk_i32 s65, 16384\n\t" \
        "LCr_%=:\n\t" \
        "v_max_u32 v200, v206, v207\n\t" \
        "v_cmp_eq_u32 vcc, -1, v200\n\t" \
        "s_cbranch_vccz LCrd_%=\n\t" \
        "buffer_load_dwordx2 v[206:207], %[vpr], %[rs], s61 offen sc1\n\t" \
        "s_memtime s[40:41]\n\t" \
        "s_memrealtime s[42:43]\n\t" \
        "s_nop 2\n\t" \
        "s_memrealtime s[44:45]\n\t" \
        "s_nop 2\n\t" \
        "s_memrealtime s[46:47]\n\t" \
        "s_nop 2\n\t" \
        "s_memrealtime s[48:49]\n\t" \
        "s_nop 2\n\t" \
        "s_memrealtime s[50:51]\n\t" \
        "s_nop 2\n\t" \
        "s_memrealtime s[52:53]\n\t" \
        "s_nop 2\n\t" \
        "s_memrealtime s[54:55]\n\t" \
        "s_nop 2\n\t" \
        "s_memrealtime s[56:57]\n\t" \
        "s_waitcnt lgkmcnt(0)\n\t" \
        "s_mov_b32 s41, 0\n\t" \
        "s_cmp_eq_u32 s44, s42\n\t" \
        "s_addc_u32 s41, s41, 0\n\t" \
        "s_cmp_eq_u32 s46, s42\n\t" \
        "s_addc_u32 s41, s41, 0\n\t" \
        "s_cmp_eq_u32 s48, s42\n\t" \
        "s_addc_u32 s41, s41, 0\n\t" \
        "s_cmp_eq_u32 s50, s42\n\t" \
        "s_addc_u32 s41, s41, 0\n\t" \
        "s_cmp_eq_u32 s52, s42\n\t" \
        "s_addc_u32 s41, s41, 0\n\t" \
        "s_cmp_eq_u32 s54, s42\n\t" \
        "s_addc_u32 s41, s41, 0\n\t" \
        "s_cmp_eq_u32 s56, s42\n\t" \
        "s_addc_u32 s41, s41, 0\n\t" \
        "s_lshl_b32 s41, s41, 4\n\t" \
        "s_add_u32 s40, s40, s41\n\t" \
        "s_mul_i32 s41, s42, 24\n\t" \
        "s_sub_u32 s40, s40, s41\n\t" \
        "s_sub_u32 s40, s40, 84\n\t" \
        "s_mov_b32 s39, s40\n\t" \
        "s_mov_b32 s63, s40\n\t" \
        "s_sub_u32 s39, s63, 60\n\t" \
        "s_sub_u32 s69, s63, 100\n\t" \
        "s_waitcnt vmcnt(0)\n\t" \
        "s_sub_u32 s65, s65, 1\n\t" \
        "s_cbranch_scc0 LCr_%=\n\t" \
        "LCrd_%=:\n\t" \
        "v_subrev_u32 v206, s67, v206\n\t" \
        "s_nop 0\n\t" \
        "v_max_i32_dpp v207, v207, v207 quad_perm:[1,0,3,2] row_mask:0xf bank_mask:0xf\n\t" \
        "v_max_i32_dpp v206, v206, v206 quad_perm:[1,0,3,2] row_mask:0xf bank_mask:0xf\n\t" \
        "s_nop 0\n\t" \
        "v_max_i32_dpp v207, v207, v207 quad_perm:[2,3,0,1] row_mask:0xf bank_mask:0xf\n\t" \
        "v_max_i32_dpp v206, v206, v206 quad_perm:[2,3,0,1] row_mask:0xf bank_mask:0xf\n\t" \
        "s_nop 0\n\t" \
        "v_max_i32_dpp v207, v207, v207 row_half_mirror row_mask:0xf bank_mask:0xf\n\t" \
        "v_max_i32_dpp v206, v206, v206 row_half_mirror row_mask:0xf bank_mask:0xf\n\t" \
        "s_nop 0\n\t" \
        "v_max_i32_dpp v207, v207, v207 row_mirror row_mask:0xf bank_mask:0xf\n\t" \
        "v_max_i32_dpp v206, v206, v206 row_mirror row_mask:0xf bank_mask:0xf\n\t" \
        "s_nop 0\n\t" \
        "v_readlane_b32 s64, v206, 0\n\t" \
        "v_readlane_b32 s73, v206, 16\n\t" \
        "s_max_i32 s64, s64, s73\n\t" \
        "v_readlane_b32 s65, v206, 32\n\t" \
        "v_readlane_b32 s73, v206, 48\n\t" \
        "s_max_i32 s64, s64, s73\n\t" \
        "s_max_i32 s64, s64, s65\n\t" \
        "s_add_u32 s62, s67, s64\n\t" \
        "v_readlane_b32 s64, v207, 32\n\t" \
        "v_readlane_b32 s73, v207, 48\n\t" \
        "s_max_i32 s64, s64, s73\n\t" \
        "s_lshr_b32 s73, s64, 20\n\t" \
        "s_and_b32 s64, s64, 0xfffff\n\t" \
        "s_andn2_b32 s77, s77, s73\n\t" \
        "s_andn2_b32 s77, s77, s76\n\t" \
        "s_andn2_b32 s73, s73, s76\n\t" \
        "s_mul_i32 s73, s73, %[inc]\n\t" \
        "s_add_u32 s74, s74, s73\n\t" \
        "s_min_u32 s74, s74, 400\n\t" \
        "s_mul_i32 s73, s77, %[inc]\n\t" \
        "s_add_u32 s75, s75, s73\n\t" \
        "s_min_u32 s75, s75, 400\n\t" \
        "s_sub_u32 s73, s62, s78\n\t" \
        "s_cmp_gt_i32 s73, 500\n\t" \
        "s_cselect_b32 s76, 1, 0\n\t" \
        "s_add_u32 s62, s62, s64\n\t" \
        "v_readlane_b32 s64, v207, 0\n\t" \
        "v_readlane_b32 s73, v207, 16\n\t" \
        "s_max_i32 s64, s64, s73\n\t" \
        "s_add_u32 s89, s62, s64\n\t" \
        "LCq_%=:\n\t" \
        "s_barrier\n\t" \
        "ds_read_b128 v[96:99], %[lz] offset:0\n\t" \
        "ds_read_b128 v[100:103], %[lz] offset:1024\n\t" \
        "ds_read_b128 v[104:107], %[lz] offset:2048\n\t" \
        "ds_read_b128 v[108:111], %[lz] offset:3072\n\t" \
        "LCw_%=:\n\t" \
        "s_movk_i32 s65, 4096\n\t" \
        "s_add_u32 s94, s62, s39\n\t" \
        "1:\n\t" \
        "s_memtime s[92:93]\n\t" \
        "s_sub_u32 s65, s65, 1\n\t" \
        "s_cbranch_scc1 3f\n\t" \
        "s_waitcnt lgkmcnt(0)\n\t" \
        "s_sub_u32 s64, s94, s92\n\t" \
        "s_cmp_gt_i32 s64, 384\n\t" \
        "s_cbranch_scc1 1b\n\t" \
        "s_cmp_lt_i32 s64, -300\n\t" \
        "s_cbranch_scc1 LCh_%=\n\t" \
        "s_sub_i32 s64, s64, 136\n\t" \
        "s_max_i32 s64, s64, 0\n\t" \
        "s_and_b32 s64, s64, 0xfc\n\t" \
        "s_getpc_b64 s[92:93]\n\t" \
        "2:\n\t" \
        "s_sub_u32 s64, 3f-2b, s64\n\t" \
        "s_add_u32 s92, s92, s64\n\t" \
        "s_addc_u32 s93, s93, 0\n\t" \
        "s_setpc_b64 s[92:93]\n\t" \
        "s_nop 0\n\t" \
        "s_nop 0\n\t" \
        "s_nop 0\n\t" \
        "s_nop 0\n\t" \
        "s_nop 0\n\t" \
        "s_nop 0\n\t" \
        "s_nop 0\n\t" \
        "s_nop 0\n\t" \
        "s_nop 0\n\t" \
        "s_nop 0\n\t" \
        "s_nop 0\n\t" \
        "s_nop 0\n\t" \
        "s_nop 0\n\t" \
        "s_nop 0\n\t" \
        "s_nop 0\n\t" \
        "s_nop 0\n\t" \
        "s_nop 0\n\t" \
        "s_nop 0\n\t" \
        "s_nop 0\n\t" \
        "s_nop 0\n\t" \
        "s_nop 0\n\t" \
        "s_nop 0\n\t" \
        "s_nop 0\n\t" \
        "s_nop 0\n\t" \
        "s_nop 0\n\t" \
        "s_nop 0\n\t" \
        "s_nop 0\n\t" \
        "s_nop 0\n\t" \
        "s_nop 0\n\t" \
        "s_nop 0\n\t" \
        "s_nop 0\n\t" \
        "s_nop 0\n\t" \
        "s_nop 0\n\t" \
        "s_nop 0\n\t" \
        "s_nop 0\n\t" \
        "s_nop 0\n\t" \
        "s_nop 0\n\t" \
        "s_nop 0\n\t" \
        "s_nop 0\n\t" \
        "s_nop 0\n\t" \
        "s_nop 0\n\t" \
        "s_nop 0\n\t" \
        "s_nop 0\n\t" \
        "s_nop 0\n\t" \
        "s_nop 0\n\t" \
        "s_nop 0\n\t" \
        "s_nop 0\n\t" \
        "s_nop 0\n\t" \
        "s_nop 0\n\t" \
        "s_nop 0\n\t" \
        "s_nop 0\n\t" \
        "s_nop 0\n\t" \
        "s_nop 0\n\t" \
        "s_nop 0\n\t" \
        "s_nop 0\n\t" \
        "s_nop 0\n\t" \
        "s_nop 0\n\t" \
        "s_nop 0\n\t" \
        "s_nop 0\n\t" \
        "s_nop 0\n\t" \
        "s_nop 0\n\t" \
        "s_nop 0\n\t" \
        "s_nop 0\n\t" \
        "s_nop 0\n\t" \
        "3:\n\t" \
        "s_waitcnt lgkmcnt(0)\n\t" \
        "buffer_load_dwordx4 v[112:115], %[l16], %[rs], s86 offen offset:0 sc1\n\t" \
        "buffer_load_dwordx4 v[116:119], %[l16], %[rs], s86 offen offset:1024 sc1\n\t" \
        "buffer_load_dwordx4 v[120:123], %[l16], %[rs], s86 offen offset:2048 sc1\n\t" \
        "buffer_load_dwordx4 v[124:127], %[l16], %[rs], s86 offen offset:3072 sc1\n\t" \
        "buffer_load_dwordx4 v[128:131], %[l16], %[rs], s87 offen offset:0 sc1\n\t" \
        "buffer_load_dwordx4 v[132:135], %[l16], %[rs], s87 offen offset:1024 sc1\n\t" \
        "buffer_load_dwordx4 v[136:139], %[l16], %[rs], s87 offen offset:2048 sc1\n\t" \
        "buffer_load_dwordx4 v[140:143], %[l16], %[rs], s87 offen offset:3072 sc1\n\t" \
        "buffer_load_dwordx4 v[144:147], %[vpm], %[rs], s88 offen offset:0 sc1\n\t" \
        "buffer_load_dwordx4 v[148:151], %[vpm], %[rs], s88 offen offset:1024 sc1\n\t" \
        "buffer_load_dwordx4 v[152:155], %[l16], %[rs], s90 offen offset:0 sc1\n\t" \
        "buffer_load_dwordx4 v[156:159], %[l16], %[rs], s90 offen offset:1024 sc1\n\t" \
        "buffer_load_dwordx4 v[160:163], %[l16], %[rs], s90 offen offset:2048 sc1\n\t" \
        "buffer_load_dwordx4 v[164:167], %[l16], %[rs], s90 offen offset:3072 sc1\n\t" \
        "buffer_load_dwordx4 v[168:171], %[l16], %[rs], s98 offen offset:0 sc1\n\t" \
        "buffer_load_dwordx4 v[172:175], %[l16], %[rs], s98 offen offset:1024 sc1\n\t" \
        "buffer_load_dwordx4 v[176:179], %[l16], %[rs], s98 offen offset:2048 sc1\n\t" \
        "buffer_load_dwordx4 v[180:183], %[l16], %[rs], s98 offen offset:3072 sc1\n\t" \
        "buffer_load_dwordx4 v[184:187], %[l16], %[rs], s99 offen offset:0 sc1\n\t" \
        "buffer_load_dwordx4 v[188:191], %[l16], %[rs], s99 offen offset:1024 sc1\n\t" \
        "buffer_load_dwordx4 v[192:195], %[l16], %[rs], s99 offen offset:2048 sc1\n\t" \
        "s_waitcnt vmcnt(20)\n\t" \
        "v_smfmac_f32_16x16x64_bf16 v[196:199], v[112:115], v[32:39], %[idx]\n\t" \
        "s_waitcnt vmcnt(19)\n\t" \
        "v_smfmac_f32_16x16x64_bf16 v[196:199], v[116:119], v[40:47], %[idx]\n\t" \
        "s_waitcnt vmcnt(18)\n\t" \
        "v_smfmac_f32_16x16x64_bf16 v[196:199], v[120:123], v[48:55], %[idx]\n\t" \
        "s_waitcnt vmcnt(17)\n\t" \
        "v_smfmac_f32_16x16x64_bf16 v[196:199], v[124:127], v[56:63], %[idx]\n\t" \
        "s_waitcnt vmcnt(16)\n\t" \
        "v_smfmac_f32_16x16x64_bf16 v[196:199], v[128:131], v[64:71], %[idx]\n\t" \
        "s_waitcnt vmcnt(15)\n\t" \
        "v_smfmac_f32_16x16x64_bf16 v[196:199], v[132:135], v[72:79], %[idx]\n\t" \
        "s_waitcnt vmcnt(14)\n\t" \
        "v_smfmac_f32_16x16x64_bf16 v[196:199], v[136:139], v[80:87], %[idx]\n\t" \
        "s_waitcnt vmcnt(13)\n\t" \
        "v_smfmac_f32_16x16x64_bf16 v[196:199], v[140:143], v[88:95], %[idx]\n\t" \
        "s_waitcnt vmcnt(12)\n\t" \
        "v_smfmac_f32_16x16x64_bf16 v[196:199], v[144:147], v[96:103], %[idx]\n\t" \
        "s_waitcnt vmcnt(11)\n\t" \
        "v_smfmac_f32_16x16x64_bf16 v[196:199], v[148:151], v[104:111], %[idx]\n\t" \
        "s_nop 8\n\t" \
        "v_pk_add_f32 v[200:201], v[196:197], v[198:199]\n\t" \
        "v_cvt_pk_bf16_f32 v202, v200, v201\n\t" \
        "v_cmp_u_f32 vcc, v200, v201\n\t" \
        "s_nop 0\n\t" \
        "v_mov_b32_dpp v203, v202 quad_perm:[1,0,3,2] row_mask:0xf bank_mask:0xf\n\t" \
        "v_perm_b32 v208, v203, v202, %[psel]\n\t" \
        "s_cbranch_vccnz LCf_%=\n\t" \
        "LCok_%=:\n\t" \
        "buffer_store_dword v208, %[vout], %[rs], s68 offen sc1\n\t" \
        "s_memtime s[92:93]\n\t" \
        "buffer_store_dword v208, %[vout], %[rs], s91 offen sc1\n\t" \
        "s_cmp_gt_u32 s66, 1\n\t" \
        "s_cbranch_scc1 LCba_%=\n\t" \
        "s_barrier\n\t" \
        "LCba_%=:\n\t" \
        "s_add_u32 s73, s68, %[po]\n\t" \
        "s_waitcnt lgkmcnt(0)\n\t" \
        "s_sub_u32 s67, s92, s69\n\t" \
        "s_sub_u32 s64, s67, s62\n\t" \
        "s_add_u32 s65, s38, 48\n\t" \
        "s_min_i32 s64, s64, s65\n\t" \
        "s_sub_u32 s65, s38, s64\n\t" \
        "s_ashr_i32 s65, s65, 2\n\t" \
        "s_sub_u32 s65, s38, s65\n\t" \
        "s_sub_u32 s65, s65, %[dec]\n\t" \
        "s_max_i32 s38, s64, s65\n\t" \
        "s_max_i32 s38, s38, 64\n\t" \
        "s_add_u32 s64, s38, %[kc]\n\t" \
        "s_add_u32 s64, s64, s74\n\t" \
        "v_mov_b32 v205, s64\n\t" \
        "s_min_u32 s77, s66, 1\n\t" \
        "s_mov_b32 s78, s89\n\t" \
        "s_sub_u32 s64, s89, s67\n\t" \
        "s_cmp_ge_i32 s64, %[dok]\n\t" \
        "s_cselect_b32 s95, 0, s95\n\t" \
        "s_cbranch_scc1 LCu_%=\n\t" \
        "s_movk_i32 s65, 4096\n\t" \
        "LCt_%=:\n\t" \
        "s_add_u32 s89, s89, 1152\n\t" \
        "s_sub_u32 s65, s65, 1\n\t" \
        "s_cbranch_scc1 LCv_%=\n\t" \
        "s_sub_u32 s64, s89, s67\n\t" \
        "s_cmp_lt_i32 s64, 230\n\t" \
        "s_cbranch_scc1 LCt_%=\n\t" \
        "LCv_%=:\n\t" \
        "s_add_u32 s89, s89, %[late]\n\t" \
        "s_add_u32 s89, s89, s95\n\t" \
        "s_movk_i32 s95, 1152\n\t" \
        "LCu_%=:\n\t" \
        "s_add_u32 s64, s89, s75\n\t" \
        "s_min_u32 s64, s64, -2\n\t" \
        "v_mov_b32 v204, s64\n\t" \
        "buffer_store_dwordx2 v[204:205], off, %[rs], s73 sc1\n\t" \
        "s_cmp_gt_u32 s66, 1\n\t" \
        "s_cbranch_scc1 LCbb_%=\n\t" \
        "LCbc_%=:\n\t" \
        "s_mov_b32 s60, s72\n\t" \
        "s_cmp_lt_u32 s60, 63\n\t" \
        "s_cbranch_scc1 LCs0_%=\n\t" \
        "s_branch LCend_%=\n\t" \
        "LCbb_%=:\n\t" \
        "s_movk_i32 s65, 16384\n\t" \
        "LCbd_%=:\n\t" \
        "s_sleep 2\n\t" \
        "buffer_load_dwordx2 v[206:207], %[vpr], %[rs], s68 offen sc1\n\t" \
        "s_waitcnt vmcnt(0)\n\t" \
        "v_max_u32 v200, v206, v207\n\t" \
        "v_cmp_eq_u32 vcc, -1, v200\n\t" \
        "s_cbranch_vccz LCbe_%=\n\t" \
        "s_sub_u32 s65, s65, 1\n\t" \
        "s_cbranch_scc0 LCbd_%=\n\t" \
        "LCbe_%=:\n\t" \
        "s_barrier\n\t" \
        "s_branch LCbc_%=\n\t" \
        "LCzl_%=:\n\t" \
        "s_movk_i32 s65, 16384\n\t" \
        "LCzm_%=:\n\t" \
        "buffer_load_dword v211, %[l4], %[rs], %[zf] offen sc1\n\t" \
        "s_waitcnt vmcnt(0)\n\t" \
        "v_cmp_ne_u32 s[96:97], -1, v211\n\t" \
        "s_nop 1\n\t" \
        "s_bitcmp1_b64 s[96:97], s72\n\t" \
        "s_cbranch_scc1 LCz_%=\n\t" \
        "s_sleep 2\n\t" \
        "s_sub_u32 s65, s65, 1\n\t" \
        "s_cbranch_scc0 LCzm_%=\n\t" \
        "s_branch LCz_%=\n\t" \
        "LCf_%=:\n\t" \
        "v_min_u32 v208, -2, v208\n\t" \
        "s_cmp_eq_u32 s66, 0\n\t" \
        "s_cbranch_scc1 LCfz_%=\n\t" \
        "v_max_u32 v200, v112, v113\n\t" \
        "v_max3_u32 v200, v200, v114, v115\n\t" \
        "v_max3_u32 v200, v200, v116, v117\n\t" \
        "v_max3_u32 v200, v200, v118, v119\n\t" \
        "v_max3_u32 v200, v200, v120, v121\n\t" \
        "v_max3_u32 v200, v200, v122, v123\n\t" \
        "v_max3_u32 v200, v200, v124, v125\n\t" \
        "v_max3_u32 v200, v200, v126, v127\n\t" \
        "v_max3_u32 v200, v200, v128, v129\n\t" \
        "v_max3_u32 v200, v200, v130, v131\n\t" \
        "v_max3_u32 v200, v200, v132, v133\n\t" \
        "v_max3_u32 v200, v200, v134, v135\n\t" \
        "v_max3_u32 v200, v200, v136, v137\n\t" \
        "v_max3_u32 v200, v200, v138, v139\n\t" \
        "v_max3_u32 v200, v200, v140, v141\n\t" \
        "v_max3_u32 v200, v200, v142, v143\n\t" \
        "v_max3_u32 v200, v200, v144, v145\n\t" \
        "v_max3_u32 v200, v200, v146, v147\n\t" \
        "v_max3_u32 v200, v200, v148, v149\n\t" \
        "v_max3_u32 v200, v200, v150, v151\n\t" \
        "v_cmp_eq_u32 vcc, -1, v200\n\t" \
        "s_cbranch_vccz LCok_%=\n\t" \
        "s_cmp_gt_u32 s66, 16384\n\t" \
        "s_cbranch_scc1 LCok_%=\n\t" \
        "LCfz_%=:\n\t" \
        "v_mov_b64 v[196:197], 0\n\t" \
        "v_mov_b64 v[198:199], 0\n\t" \
        "LCh_%=:\n\t" \
        "s_add_u32 s66, s66, 1\n\t" \
        "s_memtime s[92:93]\n\t" \
        "s_waitcnt lgkmcnt(0)\n\t" \
        "s_sub_u32 s73, s92, s63\n\t" \
        "s_movk_i32 s65, 4096\n\t" \
        "LCaa_%=:\n\t" \
        "s_add_u32 s62, s62, 1056\n\t" \
        "s_sub_u32 s65, s65, 1\n\t" \
        "s_cbranch_scc1 LCaa_%=x\n\t" \
        "s_sub_u32 s64, s62, s73\n\t" \
        "s_cmp_lt_i32 s64, -200\n\t" \
        "s_cbranch_scc1 LCaa_%=\n\t" \
        "LCaa_%=x:\n\t" \
        "s_branch LCw_%=\n\t" \
        "LCend_%=:\n\t" \
        "s_waitcnt vmcnt(0) lgkmcnt(0)\n\t"
__device__ __forceinline__ void c_role(const Params& P, int c16, unsigned lb) {
    const int tid = threadIdx.x, lane = tid & 63, w = __builtin_amdgcn_readfirstlane(tid >> 6);
    const rsrc_t rs = ws_rsrc(P);
    const int rho = lane & 15, qa = lane >> 4;
    const int idx = (rho & 2) ? 0xEEEE : 0x4444;
    const int hb = w ? 5 * (w - 1) : 0;
    const unsigned a_zl = lb + ((qa >> 1) * 16 + rho) * 16 + (qa & 1) * 8;
    const unsigned a_zw = a_zl + (hb >> 1) * 512 + (hb & 1) * 4, a_zv = a_zl + ((hb + 1) >> 1) * 512 + ((hb + 1) & 1) * 4;
    const unsigned vo_out = c16 * 256 + (8 * (qa & 1) + 4 * (rho & 1) + (rho & 2) + (qa >> 1)) * 16 + (rho >> 2) * 4;
    const unsigned vo_pm = (2 * qa + ((rho >> 1) & 1)) * 128 + (rho >> 3) * 64 + (2 * ((rho >> 2) & 1) + (rho & 1)) * 16;
    const unsigned psel = (rho & 1) ? 0x03020706u : 0x05040100u;
    const unsigned vo_pr = 65536 + 4096 + ((lane & 60) == 28 ? 0 : lane) * 64;
    const unsigned tb = chunk_table(0u, lane), tsh = 2 * (lane / 20);
    if (w) wait_words(rs, min(lane, 41) * 64, CT_AB + 7168 + hb * 448);
    TL(1, 2)
    asm volatile(C_ASM_TEXT
        :
        : [rs] "s"(rs), [wb] "s"(OFF_BW + (c16 * 16 + hb) * 7u * 2048u), [w] "s"(w), [zo] "s"(OFF_ZX + c16 * 16384u), [po] "s"(65536 + 4096 + c16 * 64),
          [kc] TUN(P.KC, 185), [dok] TUN(P.DOK, 95), [inc] TUN(P.DM & 63, 24), [dec] "i"(4), [late] TUN(P.FPAD, 200), [zf] "s"(CT_ZF + c16 * 256),
          [opr] "i"(CT_INIT), [omc] "i"(OFF_MEMC), [zs] "i"(ZSLEEP),
          [idx] "v"(idx), [azw] "v"(a_zw), [azv] "v"(a_zv), [vout] "v"(vo_out), [psel] "v"(psel), [vpr] "v"(vo_pr), [l16] "v"(lane * 16), [lz] "v"(lb + lane * 16),
          [tb] "v"(tb), [tsh] "v"(tsh)
         , [l4] "v"(lane * 4), [vpm] "v"(vo_pm)
        : S_ASM_CLOB);
    TL(1, 1)
}

__device__ __forceinline__ void pf_role(const Params& P) {
    const rsrc_t rs = ws_rsrc(P);
    unsigned acc = 0;
    for (int s = 0; s < NSEG; ++s) {
        if (s >= 4) wait_words(rs, 0, OFF_MEMC + (s - 3) * 7168);
        acc += bld4s(rs, threadIdx.x * 128, OFF_XK + s * 32768u);
    }
    asm volatile("" :: "v"(acc));
}

__device__ __forceinline__ void zx_role(const Params& P, int c16, int g, unsigned lb) {
    const int tid = threadIdx.x, lane = tid & 63, w = __builtin_amdgcn_readfirstlane(tid >> 6);
    const rsrc_t rs = ws_rsrc(P), rx = x_rsrc(P);
    const unsigned vo = lane * 16;
    const int m = lane & 15, q = lane >> 4;
    u4 W[4][14];
    const unsigned wbase = OFF_BW + (c16 * 16 + 4 * w) * 7u * 2048u;
    unsigned xo[7]; ximg_off(tid, xo);
    u4 st[7];
    XLOAD(st, g)
    WAIT_B()
    TL(1, 30)
#pragma unroll
    for (int u = 0; u < 4; ++u)
#pragma unroll
        for (int f = 0; f < 14; ++f) W[u][f] = bld16s(rs, vo + (f & 3) * 1024, wbase + (u * 14 + (f & ~3)) * 1024);
    SCHED();
    XSTORE(st, lb)
#pragma unroll
    for (int u = 0; u < 4; ++u)
#pragma unroll
        for (int f = 0; f < 14; ++f) PINA(W[u][f]);
    __syncthreads();
    TL(1, 1)
    const int nn = g == 0 ? 22 : 21;
    const unsigned xa = lb + (16 * (m >> 3) + (m & 7)) * XROW + q * 16;
#pragma nounroll
    for (int it = 0; it < nn; ++it) {
        const int s = g + 3 * it;
        XLOAD(st, min(s + 3, 63))
        SCHED();
        const unsigned xc = xa + (it & 1) * XIMG;
        f4 acc[4][2];
#pragma unroll
        for (int u = 0; u < 4; ++u) { acc[u][0] = F4Z; acc[u][1] = F4Z; }
        u4 xr[3][2];
#pragma unroll
        for (int f = 0; f < 2; ++f) { xr[f][0] = *LDSP(u4, xc + f * 64); xr[f][1] = *LDSP(u4, xc + 8 * XROW + f * 64); }
#pragma unroll
        for (int f = 0; f < 14; ++f) {
            if (f + 2 < 14) { xr[(f + 2) % 3][0] = *LDSP(u4, xc + (f + 2) * 64); xr[(f + 2) % 3][1] = *LDSP(u4, xc + 8 * XROW + (f + 2) * 64); }
            SCHED();
#pragma unroll
            for (int u = 0; u < 4; ++u) { acc[u][0] = mfma32(xr[f % 3][0], W[u][f], acc[u][0]); acc[u][1] = mfma32(xr[f % 3][1], W[u][f], acc[u][1]); }
            SCHED();
        }
        const unsigned zo = OFF_ZX + (s * 28 + c16) * 16384u + (4 * w) * 1024u;
#pragma unroll
        for (int u = 0; u < 4; ++u) {
            const f4 X = acc[u][0], Y = acc[u][1];
            bst16s(rs, vo + u * 1024, zo, (u4){pk2(X[0], X[1]), pk2(Y[0], Y[1]), pk2(X[2], X[3]), pk2(Y[2], Y[3])});
        }
        XSTORE(st, lb + ((it + 1) & 1) * XIMG)
        publish(rs, CT_ZF + (c16 * 64 + s) * 4);
        TL(1, 2 + it)
    }
    const unsigned ma = lb + (m & 7) * XROW + q * 16;
#pragma nounroll
    for (int it = 0; it < nn; ++it) {
        const int s = g + 3 * it;
        u4 zf[4];
#pragma unroll
        for (int u = 0; u < 4; ++u) zf[u] = bld16s(rs, vo + u * 1024, OFF_ZX + (s * 28 + c16) * 16384u + (4 * w) * 1024u);
        const int tok = s * 32 + 16 * (m >> 3) + 8 * (w & 1) + (m & 7);
        const unsigned vp = lane < 4 ? CT_XF + s * 64 + (4 * w + lane) * 4 : CT_AF + s * 64;
        const unsigned fl = bld4s(rs, vp, 0);
        mem_to_rows(rs, s, w, lane, lb);
        u4 pf[4][2], pm[2]; f4 av; float og;
#define LDP() { _Pragma("unroll") for (int u = 0; u < 4; ++u) { pf[u][0] = bld16s(rs, vo, OFF_PX + ((s * 16 + 4 * w + u) * 2) * 1024u); pf[u][1] = bld16s(rs, vo + 1024, OFF_PX + ((s * 16 + 4 * w + u) * 2) * 1024u); } \
            pm[0] = bld16s(rs, vo, OFF_PM + (s * 4 + w) * 2048u); pm[1] = bld16s(rs, vo + 1024, OFF_PM + (s * 4 + w) * 2048u); \
            av = __builtin_bit_cast(f4, bld16s(rs, vo, OFF_AF + ((s * 28 + c16) * 2 + (w & 1)) * 1024u)); og = __uint_as_float(bld4s(rs, tok * 4, OFF_GT)); }
        const bool miss = __any(fl == SENT);
        MEMBAR();
        if (!miss) LDP()
        SCHED();
        asm volatile("s_waitcnt lgkmcnt(0)\n\ts_barrier" ::: "memory");
        if (c16 == 0 && s == 1 && tid == 0) bst8s(rs, 0, OFF_EP, epoch_tag(rs));
        f4 c[4];
#pragma unroll
        for (int u = 0; u < 4; ++u) c[u] = F4Z;
#pragma unroll
        for (int f = 0; f < 14; ++f) {
            const u4 mf = *LDSP(u4, ma + f * 64);
#pragma unroll
            for (int u = 0; u < 4; ++u) c[u] = mfma32(mf, W[u][f], c[u]);
        }
        const f4 ca = q < 2 ? c[0] : c[2], cb = q < 2 ? c[1] : c[3];
        const u4 zm = {pk2(ca[0], ca[1]), pk2(ca[2], ca[3]), pk2(cb[0], cb[1]), pk2(cb[2], cb[3])};
        if (miss) {
            if (s < 63) { wait_words(rs, vp, 0); LDP() }
            else {
                wait_words(rs, CT_AF + s * 64, 0);
#pragma nounroll
                for (int spin = 0; spin < SPIN_MAX; ++spin) {
                    MEMBAR();
                    LDP()
                    unsigned vm = max(umax4(pm[0]), umax4(pm[1]));
#pragma unroll
                    for (int u = 0; u < 4; ++u) vm = max(vm, max(umax4(pf[u][0]), umax4(pf[u][1])));
                    if (!__any(vm == SENT)) break;
                }
            }
        }
        f4 o0 = F4Z, o1 = F4Z;
#pragma unroll
        for (int u = 0; u < 4; ++u) { o0 = mfma32(zf[u], pf[u][0], o0); o1 = mfma32(zf[u], pf[u][1], o1); }
        o0 = mfma32(zm, pm[0], o0); o1 = mfma32(zm, pm[1], o1);
        LAS f4* red = LDSP(f4, lb + 16384 + vo);
        red[(w * 2) * 64] = o0; red[(w * 2 + 1) * 64] = o1;
        asm volatile("s_waitcnt lgkmcnt(0)\n\ts_barrier" ::: "memory");
        if (w < 2) {
            const f4 bsum = (red[w * 64] + red[(2 + w) * 64]) + (red[(4 + w) * 64] + red[(6 + w) * 64]);
            const f4 r = av + bsum * og;
            bst8s(__builtin_amdgcn_make_buffer_rsrc(P.out, 0, 2048 * 896, 0x00020000), tok * 896 + q * 8, c16 * 32, (u2){pk2(r[0], r[1]), pk2(r[2], r[3])});
        }
        TL(1, 32 + it)
    }
}

__device__ __forceinline__ void x1_role(const Params& P, int h, int g, unsigned lb) {
    const int tid = threadIdx.x, lane = tid & 63, w = __builtin_amdgcn_readfirstlane(tid >> 6);
    const rsrc_t rs = ws_rsrc(P);
    const unsigned vo = lane * 16;
    constexpr unsigned L_X = 98304, L_RED = L_X + 40 * XROW;
    const unsigned wbase = OFF_AW + (h * 4 + w) * 49u * 2048u;
    const rsrc_t rx = x_rsrc(P);
    unsigned xo[7]; ximg_off(tid, xo);
    u4 st[7];
    XLOAD(st, g)
    LAS u4* lw = LDSP(u4, lb + w * 24576 + vo);
    u4 W[6][14];
    {
        u4 tl[24];
        __builtin_amdgcn_s_sleep(64);
        wait_words(rs, min(lane, 6) * 64, CT_AB + h * 448);
#pragma unroll
        for (int f = 0; f < 24; ++f) tl[f] = bld16s(rs, vo, wbase + (f < 14 ? 3 * 14 + f : 6 * 14 + f - 14) * 1024);
#pragma unroll
        for (int i = 0; i < 6; ++i)
#pragma unroll
            for (int j = 0; j < 14; ++j) {
                if (i == 5 && j < 10) continue;
                W[i][j] = bld16s(rs, vo + (j & 3) * 1024, wbase + ((i < 3 ? i : i + 1) * 14 + (j & ~3)) * 1024);
            }
        SCHED();
#pragma unroll
        for (int f = 0; f < 24; ++f) lw[f * 64] = tl[f];
#pragma unroll
        for (int i = 0; i < 48; ++i) PINA(W[i / 14][i % 14]);
    }
    const int m = lane & 15, q = lane >> 4;
    const unsigned xa0 = lb + L_X + (16 * (m >> 3) + (m & 7)) * XROW + (q & 1) * 16;
    unsigned kof[3];
#pragma unroll
    for (int x = 0; x < 3; ++x) { const int ga = 4 * x + q, set = ga >= 6, gp = ga - 6 * set; kof[x] = (7 * w + 2 * (gp >> 1) + 1) * 32 + (gp & 1) * 16 + set * 4; }
    const unsigned ko0 = (7 * w + 2 * q) * 32;
    TL(1, 1)
    for (int s = g; s < NSEG; s += 4) {
        XSTORE(st, lb + L_X)
        __syncthreads();
        XLOAD(st, min(s + 4, 63))
        SCHED();
        f4 acc[7][2];
#pragma unroll
        for (int lt = 0; lt < 7; ++lt) { acc[lt][0] = F4Z; acc[lt][1] = F4Z; }
        int beta = 7 * w + (q >> 1);
        u4 xr[2][4];
#define X1LD(bu, j) { xr[bu][0] = *LDSP(u4, xa0 + beta * 32); xr[bu][1] = *LDSP(u4, xa0 + beta * 32 + 8 * XROW); xr[bu][2] = lw[(j) * 64]; if ((j) < 10) xr[bu][3] = lw[(14 + (j)) * 64]; \
            beta += 2; beta = beta >= 28 ? beta - 28 : beta; }
        X1LD(0, 0)
#pragma unroll
        for (int j = 0; j < 14; ++j) {
            if (j + 1 < 14) X1LD((j + 1) & 1, j + 1)
            SCHED();
#pragma unroll
            for (int lt = 0; lt < 7; ++lt) {
                const u4 aw = lt == 3 ? xr[j & 1][2] : (lt == 6 && j < 10) ? xr[j & 1][3] : W[lt < 3 ? lt : lt - 1][j];
                acc[lt][0] = mfma32(aw, xr[j & 1][0], acc[lt][0]); acc[lt][1] = mfma32(aw, xr[j & 1][1], acc[lt][1]);
            }
            SCHED();
        }
        u4 F[2][4];
#pragma unroll
        for (int tt = 0; tt < 2; ++tt) {
            F[tt][0] = (u4){pk2(acc[0][tt][0], acc[0][tt][1]), pk2(acc[4][tt][0], acc[4][tt][1]), pk2(acc[1][tt][0], acc[1][tt][1]), pk2(acc[5][tt][0], acc[5][tt][1])};
            F[tt][1] = (u4){pk2(acc[0][tt][2], acc[0][tt][3]), pk2(acc[4][tt][2], acc[4][tt][3]), pk2(acc[1][tt][2], acc[1][tt][3]), pk2(acc[5][tt][2], acc[5][tt][3])};
            F[tt][2] = (u4){pk2(acc[2][tt][0], acc[2][tt][2]), pk2(acc[2][tt][1], acc[2][tt][3]), pk2(acc[3][tt][0], acc[3][tt][2]), pk2(acc[3][tt][1], acc[3][tt][3])};
            F[tt][3] = (u4){pk2(acc[6][tt][0], acc[6][tt][2]), pk2(acc[6][tt][1], acc[6][tt][3]), 0u, 0u};
        }
        LAS f4* red = LDSP(f4, lb + L_RED + w * 6144 + vo);
#pragma unroll
        for (int kt = 0; kt < 3; ++kt) {
            if (kt == 2) { TL(1, 2 + (s >> 2) * 3) mem_to_rows(rs, s, w, lane, lb + L_X + 32 * XROW); MEMBAR(); }
            const unsigned rb = lb + L_X + (kt < 2 ? 16 * kt + m : 32 + (m & 7)) * XROW;
            u4 kx[4];
            kx[0] = *LDSP(u4, rb + ko0); kx[1] = *LDSP(u4, rb + ko0 + 16);
            kx[2] = (u4){*LDSP(unsigned, rb + kof[0]), *LDSP(unsigned, rb + kof[0] + 8), *LDSP(unsigned, rb + kof[1]), *LDSP(unsigned, rb + kof[1] + 8)};
            kx[3] = (u4){*LDSP(unsigned, rb + kof[2]), *LDSP(unsigned, rb + kof[2] + 8), 0u, 0u};
#pragma unroll
            for (int tt = 0; tt < 2; ++tt) {
                f4 sc = F4Z;
#pragma unroll
                for (int f = 0; f < 4; ++f) sc = mfma32(kx[f], F[tt][f], sc);
                red[(kt * 2 + tt) * 64] = sc;
            }
        }
        __syncthreads();
        TL(1, 3 + (s >> 2) * 3)
        {
            const int tk = tid >> 3, kq = tid & 7, tt = tk >> 4, mm = tk & 15;
            const unsigned ax = lb + L_RED + ((kq >> 2) * 2 + tt) * 1024 + ((kq & 3) * 16 + mm) * 16;
            const unsigned am = lb + L_RED + (4 + tt) * 1024 + ((kq >> 2) * 16 + mm) * 16 + (kq & 3) * 4;
            f4 sx = (*LDSP(f4, ax) + *LDSP(f4, ax + 6144)) + (*LDSP(f4, ax + 12288) + *LDSP(f4, ax + 18432));
            const float sm = (*LDSP(float, am) + *LDSP(float, am + 6144)) + (*LDSP(float, am + 12288) + *LDSP(float, am + 18432));
            const int tok = 16 * (mm >> 3) + 8 * tt + (mm & 7);
#pragma unroll
            for (int r = 0; r < 4; ++r) sx[r] = (4 * kq + r <= tok) ? sx[r] : -1.0e9f * LOG2E;
            float mx = fmaxf(fmaxf(fmaxf(sx[0], sx[1]), fmaxf(sx[2], sx[3])), sm);
            mx = fmaxf(mx, dppf<DPP_X1>(mx)); mx = fmaxf(mx, dppf<DPP_X2>(mx)); mx = fmaxf(mx, dppf<DPP_HM>(mx));
            f4 e;
#pragma unroll
            for (int r = 0; r < 4; ++r) e[r] = __builtin_amdgcn_exp2f(sx[r] - mx);
            float em = __builtin_amdgcn_exp2f(sm - mx);
            float l = (e[0] + e[1]) + (e[2] + e[3]) + em;
            l += dppf<DPP_X1>(l); l += dppf<DPP_X2>(l); l += dppf<DPP_HM>(l);
            const float rl = __builtin_amdgcn_rcpf(l);
            e *= rl; em *= rl;
            const float eo = dppf<DPP_X1>(em);
            const unsigned po = tt * 1024 + ((2 * (kq >> 2) + (kq & 1)) * 16 + mm) * 16 + ((kq >> 1) & 1) * 4;
            bst4s(rs, po, OFF_PX + (s * 16 + h) * 2048u, pk2(e[0], e[1]) & 0x7FFF7FFFu);
            bst4s(rs, po + 8, OFF_PX + (s * 16 + h) * 2048u, pk2(e[2], e[3]) & 0x7FFF7FFFu);
            bst4s(rs, tt * 1024 + (((h & 2) + (kq >> 2)) * 16 + mm) * 16 + (2 * (h & 1) + ((kq >> 1) & 1)) * 4, OFF_PM + (s * 4 + (h >> 2)) * 2048u, ((kq & 1) ? pk2(eo, em) : pk2(em, eo)) & 0x7FFF7FFFu);
        }
        publish(rs, CT_XF + (s * 16 + h) * 4);
        TL(1, 4 + (s >> 2) * 3)
    }
}

__device__ __forceinline__ void wf_role(const Params& P, int p, unsigned lb) {
    const int tid = threadIdx.x, lane = tid & 63, w = __builtin_amdgcn_readfirstlane(tid >> 6), n = lane & 15, q = lane >> 4;
    const rsrc_t rs = ws_rsrc(P);
    unsigned xo[7]; ximg_off(tid, xo);
    const unsigned ta = lb + (n >> 2) * XROW + (lane & 3) * 8;
    for (int un = 43 - p; un < 58; un += 44) {
        u4 st[7];
        if (un < 42) {
            const int f = un / 3, part = un % 3;
#pragma unroll
            for (int i = 0; i < 7; ++i) { const int c = tid + 256 * i; st[i] = *(const u4*)(P.wqkv + (size_t)(32 * f + c / 56) * 1344 + part * 448 + (c % 56) * 8); }
            XSTORE(st, lb)
            __syncthreads();
#pragma unroll
            for (int i = 0; i < 8; ++i) {
                const int head = 4 * w + (i >> 1), dt = i & 1;
                const unsigned ad = ta + 8 * q * XROW + head * 56 + dt * 32;
                const u2 r0 = trread(ad), r1 = trread(ad + 4 * XROW);
                u4 v = {r0.x, r0.y, r1.x, r1.y};
                if (dt && n >= 12) v = (u4){0u, 0u, 0u, 0u};
                bst16s(rs, lane * 16, OFF_WF + ((head * 6 + part * 2 + dt) * 14 + f) * 1024u, v);
            }
        } else {
            const int head = un - 42;
#pragma unroll
            for (int i = 0; i < 7; ++i) st[i] = *(const u4*)(P.wo + (size_t)head * 28 * 448 + min(tid + 256 * i, 1567) * 8);
            XSTORE(st, lb)
            __syncthreads();
#pragma unroll
            for (int i = 0; i < 7; ++i) {
                const unsigned ad = ta + 4 * q * XROW + (7 * w + i) * 32;
                const u2 r0 = trread(ad), r1 = trread(ad + 16 * XROW);
                bst16s(rs, lane * 16, OFF_WO + ((7 * w + i) * 16 + head) * 1024u, (u4){r0.x, r0.y, q < 3 ? r1.x : 0u, q < 3 ? r1.y : 0u});
            }
        }
        __syncthreads();
    }
    publish(rs, CT_WF + p * 4);
    TL(1, 1)
}

__device__ __forceinline__ void pa_role(const Params& P, int p, unsigned lb) {
    const int tid = threadIdx.x, lane = tid & 63, w = __builtin_amdgcn_readfirstlane(tid >> 6);
    const rsrc_t rs = ws_rsrc(P);
    const unsigned vo = lane * 16;
    const int m = lane & 15, q = lane >> 4;
    {
        const rsrc_t rx = x_rsrc(P);
        u4 X[4][14];
        {
            unsigned xo[7]; ximg_off(tid, xo);
            u4 s0[7], s1[7];
            XLOAD(s0, 2 * p) XLOAD(s1, 2 * p + 1)
            XSTORE(s0, lb) XSTORE(s1, lb + XIMG)
            __syncthreads();
            const unsigned xa = lb + (16 * (m >> 3) + (m & 7)) * XROW + q * 16;
#pragma unroll
            for (int tt = 0; tt < 4; ++tt)
#pragma unroll
                for (int f = 0; f < 14; ++f) X[tt][f] = *LDSP(u4, xa + (tt >> 1) * XIMG + (tt & 1) * 8 * XROW + f * 64);
#pragma unroll
            for (int tt = 0; tt < 4; ++tt)
#pragma unroll
                for (int f = 0; f < 14; ++f) PINA(X[tt][f]);
        }
        const float qs = 0.18898223650461362f * LOG2E;
        TL(1, 2)
        wait_words(rs, (lane < 44 ? lane : 0) * 4, CT_WF);
        TL(1, 3)
        u4 wn[2][14];
#pragma unroll
        for (int f = 0; f < 14; ++f) { wn[0][f] = bld16s(rs, vo, OFF_WF + ((24 * w) * 14 + f) * 1024u); wn[1][f] = bld16s(rs, vo, OFF_WF + ((24 * w + 1) * 14 + f) * 1024u); }
        for (int hh = 0; hh < 4; ++hh) {
            const int head = 4 * w + hh;
#pragma unroll
            for (int part = 0; part < 3; ++part) {
                const int nx = min(head * 6 + part * 2 + 2, 94);
                f4 acc[2][4];
#pragma unroll
                for (int d = 0; d < 2; ++d)
#pragma unroll
                    for (int tt = 0; tt < 4; ++tt) acc[d][tt] = F4Z;
#pragma unroll
                for (int f = 0; f < 14; ++f) {
#pragma unroll
                    for (int d = 0; d < 2; ++d)
#pragma unroll
                        for (int tt = 0; tt < 4; ++tt) acc[d][tt] = part < 2 ? mfma32(wn[d][f], X[tt][f], acc[d][tt]) : mfma32(X[tt][f], wn[d][f], acc[d][tt]);
                    SCHED();
                    wn[0][f] = bld16s(rs, vo, OFF_WF + (nx * 14 + f) * 1024u); wn[1][f] = bld16s(rs, vo, OFF_WF + ((nx + 1) * 14 + f) * 1024u);
                    SCHED();
                }
                if (part < 2) {
                    const float sc = part == 0 ? qs : 1.f;
#pragma unroll
                    for (int tt = 0; tt < 4; ++tt) {
                        const f4 A = acc[0][tt] * sc, B = acc[1][tt] * sc;
                        bst16s(rs, vo, (part ? OFF_KF : OFF_QF) + ((4 * p + tt) * 16 + head) * 1024u, (u4){pk2(A[0], A[1]), pk2(A[2], A[3]), pk2(B[0], B[1]), pk2(B[2], B[3])});
                    }
                } else {
#pragma unroll
                    for (int gi = 0; gi < 2; ++gi)
#pragma unroll
                        for (int d = 0; d < 2; ++d) {
                            const f4 A = acc[d][2 * gi], B = acc[d][2 * gi + 1];
                            u4 vv = {pk2(A[0], A[1]), pk2(A[2], A[3]), pk2(B[0], B[1]), pk2(B[2], B[3])};
                            if (d == 1 && m == 12) vv = (u4){0x3F803F80u, 0x3F803F80u, 0x3F803F80u, 0x3F803F80u};
                            bst16s(rs, vo, OFF_VF + (((2 * p + gi) * 16 + head) * 2 + d) * 1024u, vv);
                        }
                }
            }
        }
        publish(rs, CT_QF + p * 4);
        TL(1, 4)
    }
    wait_words(rs, (lane & 31) * 4, CT_QF);
    LAS u4* of = LDSP(u4, lb + vo);
    LAS float* gl = LDSP(float, lb + 32768);
    u4 K[18], V[9][2], Q[2];
#define LDV(sg, hd) { _Pragma("unroll") for (int gi = 0; gi < 9; ++gi) { const int gg = max((sg) - 8 + gi, 0); \
            V[gi][0] = bld16s(rs, vo, OFF_VF + ((gg * 16 + (hd)) * 2) * 1024u); V[gi][1] = bld16s(rs, vo + 1024, OFF_VF + ((gg * 16 + (hd)) * 2) * 1024u); } }
#define LDK(sg, hd) { Q[0] = bld16s(rs, vo, OFF_QF + ((2 * (sg)) * 16 + (hd)) * 1024u); Q[1] = bld16s(rs, vo, OFF_QF + ((2 * (sg) + 1) * 16 + (hd)) * 1024u); \
        _Pragma("unroll") for (int gi = 0; gi < 9; ++gi) { const int gg = max((sg) - 8 + gi, 0); \
            K[2 * gi] = bld16s(rs, vo, OFF_KF + ((2 * gg) * 16 + (hd)) * 1024u); K[2 * gi + 1] = bld16s(rs, vo, OFF_KF + ((2 * gg + 1) * 16 + (hd)) * 1024u); } }
    TL(1, 5)
    LDK(p, 4 * w)
    LDV(p, 4 * w)
#pragma nounroll
    for (int un = 0; un < 8; ++un) {
        const int s = p + 32 * (un >> 2), head = 4 * w + (un & 3);
        const int un1 = min(un + 1, 7), s1 = p + 32 * (un1 >> 2), head1 = 4 * w + (un1 & 3);
        f4 S[2][18];
#pragma unroll
        for (int tt = 0; tt < 2; ++tt)
#pragma unroll
            for (int kt = 0; kt < 18; ++kt) S[tt][kt] = mfma32(K[kt], Q[tt], F4Z);
        SCHED();
        LDK(s1, head1)
        SCHED();
#pragma unroll
        for (int tt = 0; tt < 2; ++tt) {
            const int ql = 16 * (m >> 3) + 8 * tt + (m & 7);
            const float NEG = -__builtin_inff();
#pragma unroll
            for (int hf = 0; hf < 2; ++hf)
#pragma unroll
                for (int r = 0; r < 4; ++r) {
                    const int kl = 16 * (q >> 1) + 8 * hf + 4 * (q & 1) + r;
                    S[tt][hf][r] = kl > ql ? S[tt][hf][r] : NEG;
                    S[tt][16 + hf][r] = kl <= ql ? S[tt][16 + hf][r] : NEG;
                }
            if (s < 8) {
#pragma unroll
                for (int gi = 0; gi < 8; ++gi)
                    if (s - 8 + gi < 0) { S[tt][2 * gi] = (f4){NEG, NEG, NEG, NEG}; S[tt][2 * gi + 1] = (f4){NEG, NEG, NEG, NEG}; }
            }
            float mx = NEG;
#pragma unroll
            for (int kt = 0; kt < 18; ++kt) mx = fmaxf(fmaxf(mx, fmaxf(S[tt][kt][0], S[tt][kt][1])), fmaxf(S[tt][kt][2], S[tt][kt][3]));
            mx = fmaxf(mx, __shfl_xor(mx, 16)); mx = fmaxf(mx, __shfl_xor(mx, 32));
            f4 o0 = F4Z, o1 = F4Z;
#pragma unroll
            for (int gi = 0; gi < 9; ++gi) {
                f4 a, b;
#pragma unroll
                for (int r = 0; r < 4; ++r) { a[r] = __builtin_amdgcn_exp2f(S[tt][2 * gi][r] - mx); b[r] = __builtin_amdgcn_exp2f(S[tt][2 * gi + 1][r] - mx); }
                const u4 pfr = {pk2(a[0], a[1]), pk2(a[2], a[3]), pk2(b[0], b[1]), pk2(b[2], b[3])};
                o0 = mfma32(V[gi][0], pfr, o0); o1 = mfma32(V[gi][1], pfr, o1);
            }
            const float rl = __builtin_amdgcn_rcpf(__shfl(o1[0], 48 + m));
            o0 *= rl; o1 *= rl;
            of[(head * 2 + tt) * 64] = (u4){pk2(o0[0], o0[1]), pk2(o0[2], o0[3]), pk2(o1[0], o1[1]), pk2(o1[2], o1[3])};
        }
        SCHED();
        LDV(s1, head1)
        SCHED();
        TL(1, 6 + un)
        if ((un & 3) != 3) continue;
        u4 wv[4][7];
        unsigned wob = OFF_WO + 7 * w * 16384u; asm volatile("" : "+s"(wob));
#pragma unroll
        for (int k = 0; k < 4; ++k)
#pragma unroll
            for (int i = 0; i < 7; ++i) wv[k][i] = bld16s(rs, vo + k * 1024, wob + i * 16384u);
        {
            const int tk = tid >> 3, part = tid & 7;
            float sg = 0.f;
#pragma unroll
            for (int i = 0; i < 7; ++i) {
                const u4 xv = *(const u4*)(P.x + (size_t)(s * 32 + tk) * 448 + part * 8 + i * 64), gv = *(const u4*)(P.gate + part * 8 + i * 64);
#pragma unroll
                for (int j = 0; j < 4; ++j) {
                    sg += __uint_as_float(xv[j] << 16) * __uint_as_float(gv[j] << 16);
                    sg += __uint_as_float(xv[j] & 0xffff0000u) * __uint_as_float(gv[j] & 0xffff0000u);
                }
            }
            sg += dppf<DPP_X1>(sg); sg += dppf<DPP_X2>(sg); sg += dppf<DPP_HM>(sg);
            const float gg = 1.f / (1.f + __expf(-sg));
            if (part == 0) { gl[tk] = gg; bst4s(rs, (s * 32 + tk) * 4, OFF_GT, __float_as_uint(1.f - gg)); }
        }
        __syncthreads();
        f4 acc[7][2];
#pragma unroll
        for (int i = 0; i < 7; ++i) { acc[i][0] = F4Z; acc[i][1] = F4Z; }
#pragma unroll
        for (int hd = 0; hd < 16; ++hd) {
            const u4 o0 = of[(hd * 2) * 64], o1 = of[(hd * 2 + 1) * 64];
#pragma unroll
            for (int i = 0; i < 7; ++i) { acc[i][0] = mfma32(wv[hd & 3][i], o0, acc[i][0]); acc[i][1] = mfma32(wv[hd & 3][i], o1, acc[i][1]); }
            SCHED();
            if (hd + 4 < 16) {
#pragma unroll
                for (int i = 0; i < 7; ++i) wv[hd & 3][i] = bld16s(rs, vo + (hd & 3) * 1024, wob + i * 16384u + ((hd + 4) >> 2) * 4096u);
            }
            SCHED();
        }
        const float g0 = gl[16 * (m >> 3) + (m & 7)], g1 = gl[16 * (m >> 3) + 8 + (m & 7)];
#pragma unroll
        for (int i = 0; i < 7; ++i) {
            bst16s(rs, vo, OFF_AF + ((s * 28 + 7 * w + i) * 2) * 1024u, __builtin_bit_cast(u4, acc[i][0] * g0));
            bst16s(rs, vo + 1024, OFF_AF + ((s * 28 + 7 * w + i) * 2) * 1024u, __builtin_bit_cast(u4, acc[i][1] * g1));
        }
        publish(rs, CT_AF + s * 64);
        TL(1, 14 + (un >> 2))
    }
}

__global__ void __launch_bounds__(256, 1) k_main(Params P) {
    P.ws = (unsigned char*)(((uintptr_t)P.ws + 2097151) & ~(uintptr_t)2097151);
    extern __shared__ __attribute__((aligned(16))) unsigned char lds[];
    const unsigned lb = (unsigned)(size_t)(LAS unsigned char*)lds;
    const int b = blockIdx.x, r = b >> 3;
    const unsigned rot = phase1(P, lb);
    __syncthreads();
    TL(1, 0)
    const int gr = b & 7, gs = GRP_S(rot), gc = GRP_C(rot);
    if (gr == gs) {
        if (r < 16) s_role(P, r, lb);
        else if (r == 16) pf_role(P);
        return;
    }
    if (gr == gc) { if (r < 28) c_role(P, r, lb); return; }
    const int hg = gr - (gr > gs) - (gr > gc);
    const int nx1 = hg == 3 ? 24 : 20;
    if (hg < 3 && r < 28) { const int k = hg * 28 + r; zx_role(P, k / 3, k % 3, lb); }
    else if (hg >= 3 && r < nx1) { const int k = (hg == 3 ? 0 : hg == 4 ? 24 : 44) + r; x1_role(P, k >> 2, k & 3, lb); }
    else {
        const int p = hg < 3 ? 32 + hg * 4 + r - 28 : (hg == 3 ? 0 : hg == 4 ? 8 : 20) + r - nx1;
        wf_role(P, p, lb);
        if (p < 32) pa_role(P, p, lb);
    }
}

extern "C" void kernel_launch(void* const* d_in, const int* in_sizes, int n_in, void* d_out, int out_size, void* d_ws, size_t ws_size, hipStream_t stream) {
    Params P;
    P.x = (const unsigned short*)d_in[0]; P.wqkv = (const unsigned short*)d_in[1]; P.wo = (const unsigned short*)d_in[2]; P.mem0 = (const unsigned short*)d_in[4];
    P.Wq = (const unsigned short*)d_in[5]; P.Wk = (const unsigned short*)d_in[6]; P.Wv = (const unsigned short*)d_in[7]; P.Wo = (const unsigned short*)d_in[8];
    P.gate = (const unsigned short*)d_in[10]; P.out = (unsigned short*)d_out;
    P.ws = (unsigned char*)d_ws;
    (void)hipFuncSetAttribute((const void*)k_main, hipFuncAttributeMaxDynamicSharedMemorySize, LDS_BYTES);
    k_main<<<256, 256, LDS_BYTES, stream>>>(P);
}
```
